```python
import torch

def _softmax(x, axis=-1):
    return torch.softmax(x, dim=axis)
def _gelu(x):
    return torch.nn.functional.gelu(x, approximate="tanh")
def _silu(x):
    return torch.nn.functional.silu(x)
def _sigmoid(x):
    return torch.sigmoid(x)
def _layernorm(x, axis=-1, eps=1e-5):
    m = x.mean(dim=axis, keepdim=True)
    v = x.var(dim=axis, keepdim=True, unbiased=False)
    return (x - m) / torch.sqrt(v + eps)
def _scatter_add(tgt, idx, src, axis=0):
    return tgt.clone().index_add_(axis, idx, src)
def _segment_sum(src, seg, n):
    out = torch.zeros((n,) + src.shape[1:], dtype=src.dtype)
    return out.index_add_(0, seg, src)
def _pad(x, pw):
    flat = tuple(v for pair in reversed(pw) for v in pair)
    return torch.nn.functional.pad(x, flat)

kernel_name = 'sequential_preLN__ssm_conv_s4__dense_activation_relu__prompt'

def setup_inputs():

    _w = (lambda a: (lambda t: t.to(torch.bfloat16) if t.is_floating_point() else t)(torch.as_tensor(a)))
    def _A_setup():
        B, L, D, N = 1, 2048, 512, 128
        A_re = -(lambda _x: torch.exp(torch.as_tensor(_x)))((lambda *s: torch.randn(*s, dtype=torch.bfloat16) * 6.4)(D, N))
        A_im = torch.pi * (lambda *s: torch.randn(*s, dtype=torch.bfloat16) * 0.05)(D, N)
        B_re = (lambda *s: torch.randn(*s, dtype=torch.bfloat16) * 0.05)(D, N)
        B_im = (lambda *s: torch.randn(*s, dtype=torch.bfloat16) * 0.05)(D, N)
        C_re = (lambda *s: torch.randn(*s, dtype=torch.bfloat16) * 0.05)(D, N)
        C_im = (lambda *s: torch.randn(*s, dtype=torch.bfloat16) * 0.05)(D, N)
        log_dt = (lambda *s: torch.randn(*s, dtype=torch.bfloat16) * 0.05)(D) * 0.5 - 4.0
        Dskip = (lambda *s: torch.randn(*s, dtype=torch.bfloat16) * 0.05)(D)
        return {
            "u": _w((lambda *s: torch.randn(*s, dtype=torch.bfloat16) * 0.05)(B, L, D)),
            "A_re": _w(A_re),
            "A_im": _w(A_im),
            "B_re": _w(B_re),
            "B_im": _w(B_im),
            "C_re": _w(C_re),
            "C_im": _w(C_im),
            "log_dt": _w(log_dt),
            "Dskip": _w(Dskip),
        }
    _A_in = _A_setup()

    def _B_setup():
        B, S, D, Dff = 1, 2048, 512, 1792
        return {
            "x": _w((lambda *s: torch.randn(*s, dtype=torch.bfloat16) * 0.05)(B, S, D)),
            "w1": _w((lambda *s: torch.randn(*s, dtype=torch.bfloat16) * 0.05)(D, Dff)),
            "w2": _w((lambda *s: torch.randn(*s, dtype=torch.bfloat16) * 0.05)(Dff, D)),
        }
    _B_in = _B_setup()

    return {
        "A_u": _A_in["u"],
        "A_A_re": _A_in["A_re"],
        "A_A_im": _A_in["A_im"],
        "A_B_re": _A_in["B_re"],
        "A_B_im": _A_in["B_im"],
        "A_C_re": _A_in["C_re"],
        "A_C_im": _A_in["C_im"],
        "A_log_dt": _A_in["log_dt"],
        "A_Dskip": _A_in["Dskip"],
        "B_x": _B_in["x"],
        "B_w1": _B_in["w1"],
        "B_w2": _B_in["w2"],
    }

def reference(A_u, A_A_re, A_A_im, A_B_re, A_B_im, A_C_re, A_C_im, A_log_dt, A_Dskip, B_x, B_w1, B_w2):
    def _A_ref(u, A_re, A_im, B_re, B_im, C_re, C_im, log_dt, Dskip):
        Bsz, L, D = u.shape
        dt = torch.exp(log_dt)[:, None]
        A = A_re + 1j * A_im
        Bc = B_re + 1j * B_im
        Cc = C_re + 1j * C_im
        logdA = dt * A
        dB = dt * Bc
        l = (lambda x, dt: x.to(dt))(torch.arange(L), A_re.dtype)
        P = torch.exp(logdA[:, :, None] * l[None, None, :])
        K = torch.einsum("dn,dnl,dn->dl", Cc, P, dB).real
        L2 = 2 * L
        Kp = _pad(K, ((0, 0), (0, L)))
        up = (lambda x, *d: x.permute(*d))(u, 0, 2, 1)
        up = _pad(up, ((0, 0), (0, 0), (0, L)))
        Kf = (lambda x, axes=(-2,-1): torch.fft.rfft2(x.float(), dim=axes))(Kp[:, None, :], axes=(-2, -1))
        Uf = (lambda x, axes=(-2,-1): torch.fft.rfft2(x.float(), dim=axes))(up[:, :, None, :], axes=(-2, -1))
        Yf = Uf * Kf[None, :, :, :]
        y = (lambda x, s, axes=(-2,-1): torch.fft.irfft2(x, s=s, dim=axes).to(torch.bfloat16))(Yf, (1, L2), axes=(-2, -1))[:, :, 0, :L]
        y = (lambda x, *d: x.permute(*d))(y, 0, 2, 1) + u * Dskip[None, None, :]
        return y

    def _B_ref(x, w1, w2):
        h = torch.einsum("bsd,df->bsf", x, w1)
        zero = torch.zeros_like(h)
        h = torch.maximum(h, zero)
        return torch.einsum("bsf,fd->bsd", h, w2)

    x = A_u
    h = x + _A_ref(_layernorm(x, axis=-1), A_A_re, A_A_im, A_B_re, A_B_im, A_C_re, A_C_im, A_log_dt, A_Dskip)
    h2 = h + _B_ref(_layernorm(h, axis=-1), B_w1, B_w2)
    return h2
```

```cpp
#include <hip/hip_runtime.h>
#include <hip/hip_bf16.h>
typedef unsigned short u16;
constexpr int L=2048, D=512, N=128, F=1792;
__device__ __forceinline__ float bf2f(u16 x){ return __uint_as_float((unsigned)x<<16); }
__device__ __forceinline__ u16 f2bf(float f){ unsigned u=__float_as_uint(f); return (u16)((u+0x7fff+((u>>16)&1))>>16); }
__device__ __forceinline__ float rb(float f){ return bf2f(f2bf(f)); }

__global__ void ln_kernel(const u16* x, u16* u){
  int row=blockIdx.x; int t=threadIdx.x;
  float v[8]; float s=0;
  for(int j=0;j<8;j++){ v[j]=bf2f(x[row*D+t*8+j]); s+=v[j]; }
  for(int o=32;o;o>>=1) s+=__shfl_xor(s,o);
  float m=s/D; float q=0;
  for(int j=0;j<8;j++){ float d=v[j]-m; q+=d*d; }
  for(int o=32;o;o>>=1) q+=__shfl_xor(q,o);
  float mb=rb(m), vb=rb(q/D); float sb=rb(sqrtf(rb(vb+1e-5f)));
  for(int j=0;j<8;j++) u[row*D+t*8+j]=f2bf(rb(v[j]-mb)/sb);
}
__global__ void kgen(const u16* Are,const u16* Aim,const u16* Bre,const u16* Bim,const u16* Cre,const u16* Cim,const u16* logdt, float* K){
  int d=blockIdx.x;
  __shared__ float xr[N],xi[N],wr[N],wi[N];
  float dt=rb(expf(bf2f(logdt[d])));
  if(threadIdx.x<N){ int n=threadIdx.x; xr[n]=dt*bf2f(Are[d*N+n]); xi[n]=dt*bf2f(Aim[d*N+n]);
    float br=dt*bf2f(Bre[d*N+n]), bi=dt*bf2f(Bim[d*N+n]); float cr=bf2f(Cre[d*N+n]), ci=bf2f(Cim[d*N+n]);
    wr[n]=cr*br-ci*bi; wi[n]=cr*bi+ci*br; }
  __syncthreads();
  for(int l=threadIdx.x;l<L;l+=blockDim.x){ float lf=rb((float)l); float s=0;
    for(int n=0;n<N;n++){ float e=expf(xr[n]*lf); float sn,cs; sincosf(xi[n]*lf,&sn,&cs); s+=e*(wr[n]*cs-wi[n]*sn);}
    K[d*L+l]=s; }
}
__global__ void conv(const u16* x,const u16* u,const float* K,const u16* Dskip,u16* h){
  int d=blockIdx.x; __shared__ float su[L], sk[L];
  for(int l=threadIdx.x;l<L;l+=blockDim.x){ su[l]=bf2f(u[l*D+d]); sk[l]=K[d*L+l]; }
  __syncthreads();
  float ds=bf2f(Dskip[d]);
  for(int l=threadIdx.x;l<L;l+=blockDim.x){ float s=0; for(int j=0;j<=l;j++) s+=sk[j]*su[l-j];
    float y=rb(rb(s)+rb(su[l]*ds)); h[l*D+d]=f2bf(bf2f(x[l*D+d])+y); }
}
__global__ void gemm1(const u16* a,const u16* w,u16* H){ int l=blockIdx.x; __shared__ float sa[D];
  for(int k=threadIdx.x;k<D;k+=blockDim.x) sa[k]=bf2f(a[l*D+k]); __syncthreads();
  for(int f=threadIdx.x;f<F;f+=blockDim.x){ float s=0; for(int k=0;k<D;k++) s+=sa[k]*bf2f(w[k*F+f]); s=rb(s); H[l*F+f]=f2bf(s>0?s:0.f);} }
__global__ void gemm2(const u16* H,const u16* w,const u16* h,u16* out){ int l=blockIdx.x; __shared__ float sa[F];
  for(int k=threadIdx.x;k<F;k+=blockDim.x) sa[k]=bf2f(H[l*F+k]); __syncthreads();
  for(int d=threadIdx.x;d<D;d+=blockDim.x){ float s=0; for(int k=0;k<F;k++) s+=sa[k]*bf2f(w[k*D+d]); out[l*D+d]=f2bf(bf2f(h[l*D+d])+rb(s));} }

extern "C" void kernel_launch(void* const* d_in, const int* in_sizes, int n_in, void* d_out, int out_size, void* d_ws, size_t ws_size, hipStream_t stream){
  const u16* x=(const u16*)d_in[0];
  char* ws=(char*)d_ws;
  u16* u=(u16*)ws; float* K=(float*)(ws+(4<<20)); u16* h=(u16*)(ws+(12<<20)); u16* u2=(u16*)(ws+(16<<20)); u16* H=(u16*)(ws+(20<<20));
  ln_kernel<<<L,64,0,stream>>>(x,u);
  kgen<<<D,256,0,stream>>>((const u16*)d_in[1],(const u16*)d_in[2],(const u16*)d_in[3],(const u16*)d_in[4],(const u16*)d_in[5],(const u16*)d_in[6],(const u16*)d_in[7],K);
  conv<<<D,256,0,stream>>>(x,u,K,(const u16*)d_in[8],h);
  ln_kernel<<<L,64,0,stream>>>(h,u2);
  gemm1<<<L,256,0,stream>>>(u2,(const u16*)d_in[10],H);
  gemm2<<<L,256,0,stream>>>(H,(const u16*)d_in[11],h,(u16*)d_out);
}
```

```cpp
#include <hip/hip_runtime.h>
#include <hip/hip_bf16.h>
typedef unsigned short u16;
constexpr int L=2048, D=512, N=128, F=1792;
__device__ __forceinline__ float bf2f(u16 x){ return __uint_as_float((unsigned)x<<16); }
__device__ __forceinline__ u16 f2bf(float f){ unsigned u=__float_as_uint(f); return (u16)((u+0x7fff+((u>>16)&1))>>16); }
__device__ __forceinline__ float rb(float f){ return bf2f(f2bf(f)); }

__global__ void ln_kernel(const u16* x, u16* u){
  int row=blockIdx.x; int t=threadIdx.x;
  float v[8]; float s=0;
  for(int j=0;j<8;j++){ v[j]=bf2f(x[row*D+t*8+j]); s+=v[j]; }
  for(int o=32;o;o>>=1) s+=__shfl_xor(s,o);
  float m=s/D; float q=0;
  for(int j=0;j<8;j++){ float d=v[j]-m; q+=d*d; }
  for(int o=32;o;o>>=1) q+=__shfl_xor(q,o);
  float mb=rb(m), vb=rb(q/D); float sb=rb(sqrtf(rb(vb+1e-5f)));
  for(int j=0;j<8;j++) u[row*D+t*8+j]=f2bf(rb(v[j]-mb)/sb);
}
__global__ void kgen(const u16* Are,const u16* Aim,const u16* Bre,const u16* Bim,const u16* Cre,const u16* Cim,const u16* logdt, float* K){
  int d=blockIdx.x;
  __shared__ float xr[N],xi[N],wr[N],wi[N];
  float dt=rb(expf(bf2f(logdt[d])));
  if(threadIdx.x<N){ int n=threadIdx.x; xr[n]=dt*bf2f(Are[d*N+n]); xi[n]=dt*bf2f(Aim[d*N+n]);
    float br=dt*bf2f(Bre[d*N+n]), bi=dt*bf2f(Bim[d*N+n]); float cr=bf2f(Cre[d*N+n]), ci=bf2f(Cim[d*N+n]);
    wr[n]=cr*br-ci*bi; wi[n]=cr*bi+ci*br; }
  __syncthreads();
  for(int l=threadIdx.x;l<L;l+=blockDim.x){ float lf=rb((float)l); float s=0;
    for(int n=0;n<N;n++){ float e=expf(xr[n]*lf); float sn,cs; sincosf(xi[n]*lf,&sn,&cs); s+=e*(wr[n]*cs-wi[n]*sn);}
    K[d*L+l]=s; }
}
__global__ void conv(const u16* x,const u16* u,const float* K,const u16* Dskip,u16* h){
  int d=blockIdx.x; __shared__ float su[L], sk[L];
  for(int l=threadIdx.x;l<L;l+=blockDim.x){ su[l]=bf2f(u[l*D+d]); sk[l]=K[d*L+l]; }
  __syncthreads();
  float ds=bf2f(Dskip[d]);
  for(int l=threadIdx.x;l<L;l+=blockDim.x){ float s=0; for(int j=0;j<=l;j++) s+=sk[j]*su[l-j];
    float y=rb(rb(s)+rb(su[l]*ds)); h[l*D+d]=f2bf(bf2f(x[l*D+d])+y); }
}
__global__ void gemm1(const u16* a,const u16* w,u16* H){ int l=blockIdx.x; __shared__ float sa[D];
  for(int k=threadIdx.x;k<D;k+=blockDim.x) sa[k]=bf2f(a[l*D+k]); __syncthreads();
  for(int f=threadIdx.x;f<F;f+=blockDim.x){ float s=0; for(int k=0;k<D;k++) s+=sa[k]*bf2f(w[k*F+f]); s=rb(s); H[l*F+f]=f2bf(s>0?s:0.f);} }
__global__ void gemm2(const u16* H,const u16* w,const u16* h,u16* out){ int l=blockIdx.x; __shared__ float sa[F];
  for(int k=threadIdx.x;k<F;k+=blockDim.x) sa[k]=bf2f(H[l*F+k]); __syncthreads();
  for(int d=threadIdx.x;d<D;d+=blockDim.x){ float s=0; for(int k=0;k<F;k++) s+=sa[k]*bf2f(w[k*D+d]); out[l*D+d]=f2bf(bf2f(h[l*D+d])+rb(s));} }


__global__ void chase(const char* p, unsigned stride, int n, unsigned* sink){
  unsigned off=0, acc=0;
  for(int i=0;i<n;i++){ unsigned v; asm volatile("global_load_dword %0, %1, %2\n\ts_waitcnt vmcnt(0)" : "=v"(v) : "v"(off), "s"(p) : "memory"); acc+=v; off+=stride; }
  *sink=acc;
}
extern "C" void kernel_launch(void* const* d_in, const int* in_sizes, int n_in, void* d_out, int out_size, void* d_ws, size_t ws_size, hipStream_t stream){
  const u16* x=(const u16*)d_in[0];
  char* ws=(char*)d_ws;
  u16* u=(u16*)ws; float* K=(float*)(ws+(4<<20)); u16* h=(u16*)(ws+(12<<20)); u16* u2=(u16*)(ws+(16<<20)); u16* H=(u16*)(ws+(20<<20));
  chase<<<1,1,0,stream>>>((const char*)d_ws+(128<<20), 2048, 1024,(unsigned*)(ws+(60<<20)));
  ln_kernel<<<L,64,0,stream>>>(x,u);
  kgen<<<D,256,0,stream>>>((const u16*)d_in[1],(const u16*)d_in[2],(const u16*)d_in[3],(const u16*)d_in[4],(const u16*)d_in[5],(const u16*)d_in[6],(const u16*)d_in[7],K);
  conv<<<D,256,0,stream>>>(x,u,K,(const u16*)d_in[8],h);
  ln_kernel<<<L,64,0,stream>>>(h,u2);
  gemm1<<<L,256,0,stream>>>(u2,(const u16*)d_in[10],H);
  gemm2<<<L,256,0,stream>>>(H,(const u16*)d_in[11],h,(u16*)d_out);
}
```

```cpp
#include <hip/hip_runtime.h>
#include <hip/hip_bf16.h>
typedef unsigned short u16;
constexpr int L=2048, D=512, N=128, F=1792;
__device__ __forceinline__ float bf2f(u16 x){ return __uint_as_float((unsigned)x<<16); }
__device__ __forceinline__ u16 f2bf(float f){ unsigned u=__float_as_uint(f); return (u16)((u+0x7fff+((u>>16)&1))>>16); }
__device__ __forceinline__ float rb(float f){ return bf2f(f2bf(f)); }

__global__ void ln_kernel(const u16* x, u16* u){
  int row=blockIdx.x; int t=threadIdx.x;
  float v[8]; float s=0;
  for(int j=0;j<8;j++){ v[j]=bf2f(x[row*D+t*8+j]); s+=v[j]; }
  for(int o=32;o;o>>=1) s+=__shfl_xor(s,o);
  float m=s/D; float q=0;
  for(int j=0;j<8;j++){ float d=v[j]-m; q+=d*d; }
  for(int o=32;o;o>>=1) q+=__shfl_xor(q,o);
  float mb=rb(m), vb=rb(q/D); float sb=rb(sqrtf(rb(vb+1e-5f)));
  for(int j=0;j<8;j++) u[row*D+t*8+j]=f2bf(rb(v[j]-mb)/sb);
}
__global__ void kgen(const u16* Are,const u16* Aim,const u16* Bre,const u16* Bim,const u16* Cre,const u16* Cim,const u16* logdt, float* K){
  int d=blockIdx.x;
  __shared__ float xr[N],xi[N],wr[N],wi[N];
  float dt=rb(expf(bf2f(logdt[d])));
  if(threadIdx.x<N){ int n=threadIdx.x; xr[n]=dt*bf2f(Are[d*N+n]); xi[n]=dt*bf2f(Aim[d*N+n]);
    float br=dt*bf2f(Bre[d*N+n]), bi=dt*bf2f(Bim[d*N+n]); float cr=bf2f(Cre[d*N+n]), ci=bf2f(Cim[d*N+n]);
    wr[n]=cr*br-ci*bi; wi[n]=cr*bi+ci*br; }
  __syncthreads();
  for(int l=threadIdx.x;l<L;l+=blockDim.x){ float lf=rb((float)l); float s=0;
    for(int n=0;n<N;n++){ float e=expf(xr[n]*lf); float sn,cs; sincosf(xi[n]*lf,&sn,&cs); s+=e*(wr[n]*cs-wi[n]*sn);}
    K[d*L+l]=s; }
}
__global__ void conv(const u16* x,const u16* u,const float* K,const u16* Dskip,u16* h){
  int d=blockIdx.x; __shared__ float su[L], sk[L];
  for(int l=threadIdx.x;l<L;l+=blockDim.x){ su[l]=bf2f(u[l*D+d]); sk[l]=K[d*L+l]; }
  __syncthreads();
  float ds=bf2f(Dskip[d]);
  for(int l=threadIdx.x;l<L;l+=blockDim.x){ float s=0; for(int j=0;j<=l;j++) s+=sk[j]*su[l-j];
    float y=rb(rb(s)+rb(su[l]*ds)); h[l*D+d]=f2bf(bf2f(x[l*D+d])+y); }
}
__global__ void gemm1(const u16* a,const u16* w,u16* H){ int l=blockIdx.x; __shared__ float sa[D];
  for(int k=threadIdx.x;k<D;k+=blockDim.x) sa[k]=bf2f(a[l*D+k]); __syncthreads();
  for(int f=threadIdx.x;f<F;f+=blockDim.x){ float s=0; for(int k=0;k<D;k++) s+=sa[k]*bf2f(w[k*F+f]); s=rb(s); H[l*F+f]=f2bf(s>0?s:0.f);} }
__global__ void gemm2(const u16* H,const u16* w,const u16* h,u16* out){ int l=blockIdx.x; __shared__ float sa[F];
  for(int k=threadIdx.x;k<F;k+=blockDim.x) sa[k]=bf2f(H[l*F+k]); __syncthreads();
  for(int d=threadIdx.x;d<D;d+=blockDim.x){ float s=0; for(int k=0;k<F;k++) s+=sa[k]*bf2f(w[k*D+d]); out[l*D+d]=f2bf(bf2f(h[l*D+d])+rb(s));} }


typedef unsigned long long u64;
__device__ __forceinline__ u64 rt(){ u64 t; asm volatile("s_memrealtime %0\n\ts_waitcnt lgkmcnt(0)" : "=s"(t) :: "memory"); return t; }
__global__ void __launch_bounds__(256,1) skewk(u64* rec, int K){
  __shared__ char pin[100*1024];
  u64 r0=rt();
  pin[threadIdx.x]=0;
  if(threadIdx.x==0) __hip_atomic_store(rec+blockIdx.x, r0, __ATOMIC_RELAXED, __HIP_MEMORY_SCOPE_AGENT);
  u64 v; int sp=0;
  for(;sp<100000;sp++){ v=__hip_atomic_load(rec+threadIdx.x, __ATOMIC_RELAXED, __HIP_MEMORY_SCOPE_AGENT); long long d=(long long)(v-r0); if(__syncthreads_and(d>-2000 && d<2000)) break; }
  __shared__ u64 mn, mx; if(threadIdx.x==0){mn=~0ull;mx=0;} __syncthreads();
  atomicMin(&mn,v); atomicMax(&mx,v); __syncthreads();
  u64 tgt=mx+(mx-mn)*K; if(sp>=100000) tgt=0;
  while(rt()<tgt) __builtin_amdgcn_s_sleep(8);
  if(pin[(threadIdx.x+1)&255]==77) rec[300]=1;
}
extern "C" void kernel_launch(void* const* d_in, const int* in_sizes, int n_in, void* d_out, int out_size, void* d_ws, size_t ws_size, hipStream_t stream){
  const u16* x=(const u16*)d_in[0];
  char* ws=(char*)d_ws;
  u16* u=(u16*)ws; float* K=(float*)(ws+(4<<20)); u16* h=(u16*)(ws+(12<<20)); u16* u2=(u16*)(ws+(16<<20)); u16* H=(u16*)(ws+(20<<20));
  skewk<<<256,256,0,stream>>>((u64*)(ws+(60<<20)),0);
  ln_kernel<<<L,64,0,stream>>>(x,u);
  kgen<<<D,256,0,stream>>>((const u16*)d_in[1],(const u16*)d_in[2],(const u16*)d_in[3],(const u16*)d_in[4],(const u16*)d_in[5],(const u16*)d_in[6],(const u16*)d_in[7],K);
  conv<<<D,256,0,stream>>>(x,u,K,(const u16*)d_in[8],h);
  ln_kernel<<<L,64,0,stream>>>(h,u2);
  gemm1<<<L,256,0,stream>>>(u2,(const u16*)d_in[10],H);
  gemm2<<<L,256,0,stream>>>(H,(const u16*)d_in[11],h,(u16*)d_out);
}
```

```cpp
#include <hip/hip_runtime.h>
#include <hip/hip_bf16.h>

typedef unsigned short u16;
constexpr unsigned WS_HQ = 2883584u, HQS = 33536u, WS_HM = 5u << 20, WS_HS = 11u << 20, WS_DBG = 16u << 20;
constexpr unsigned LDS_BYTES = 163840;

struct Buf { const void* p; unsigned bytes, flags; };
struct Args { Buf x, ws, w2, w1, out; const void* prm[8]; };

__global__ void __launch_bounds__(256, 1) fused_kernel(Args) {
    unsigned tid = threadIdx.x;
    asm volatile(
        "\ts_load_dwordx16 s[4:19], s[0:1], 0x0\n"
        "\ts_load_dwordx16 s[20:35], s[0:1], 0x40\n"
        "\ts_load_dwordx4 s[36:39], s[0:1], 0x80\n"
        "\tv_lshrrev_b32 v63, 6, v0\n"
        "\tv_and_b32 v26, 63, v0\n"
        "\tv_readfirstlane_b32 s3, v63\n"
        "\tv_lshlrev_b32 v1, 4, v26\n"
        "\ts_and_b32 s55, s2, 7\n"
        "\ts_lshr_b32 s56, s2, 3\n"
        "\ts_lshl_b32 s57, s55, 5\n"
        "\ts_add_u32 s40, s57, s56\n"
        "\ts_lshl_b32 s57, s40, 13\n"
        "\ts_lshl_b32 s58, s3, 11\n"
        "\ts_add_u32 s45, s57, s58\n"
        "\ts_lshr_b32 s59, s40, 2\n"
        "\ts_and_b32 s60, s40, 3\n"
        "\ts_lshl_b32 s57, s59, 15\n"
        "\ts_lshl_b32 s58, s60, 8\n"
        "\ts_add_u32 s57, s57, s58\n"
        "\ts_lshl_b32 s58, s3, 6\n"
        "\ts_add_u32 s46, s57, s58\n"
        "\ts_lshl_b32 s57, s60, 6\n"
        "\ts_lshl_b32 s58, s3, 4\n"
        "\ts_add_u32 s61, s57, s58\n"
        "\ts_mul_i32 s57, s61, 8704\n"
        "\ts_lshl_b32 s58, s59, 6\n"
        "\ts_add_u32 s57, s57, s58\n"
        "\ts_add_u32 s48, s57, 0x40000\n"
        "\ts_add_u32 s51, s48, 4352\n"
        "\ts_mul_i32 s57, s59, 2304\n"
        "\ts_lshl_b32 s58, s61, 3\n"
        "\ts_add_u32 s57, s57, s58\n"
        "\ts_add_u32 s49, s57, 0x8000\n"
        "\ts_lshl_b32 s57, s3, 12\n"
        "\ts_lshl_b32 s58, s40, 4\n"
        "\ts_add_u32 s57, s57, s58\n"
        "\ts_add_u32 s50, s57, 0x1000\n"
        "\ts_lshl_b32 s57, s40, 7\n"
        "\ts_add_u32 s47, s57, 0x2c0000\n"
        "\ts_lshr_b32 s62, s3, 1\n"
        "\tv_and_b32 v63, 3, v0\n"
        "\tv_lshrrev_b32 v29, 2, v26\n"
        "\tv_lshlrev_b32 v65, 2, v29\n"
        "\tv_lshl_add_u32 v67, v63, 13, v65\n"
        "\tv_add_u32 v68, 0x1000, v67\n"
        "\tv_lshlrev_b32 v30, 3, v63\n"
        "\tv_lshlrev_b32 v65, 4, v63\n"
        "\tv_mul_u32_u24 v241, 8704, v29\n"
        "\tv_add_u32 v241, v241, v65\n"
        "\tv_lshrrev_b32 v66, 2, v0\n"
        "\tv_mul_u32_u24 v25, 33536, v66\n"
        "\tv_add_u32 v25, v25, v65\n"
        "\tv_mul_u32_u24 v228, 2304, v66\n"
        "\tv_lshlrev_b32 v242, 3, v29\n"
        "\tv_mov_b32 v66, 0x40000000\n"
        "\tv_cmp_eq_u32 vcc, 0, v63\n"
        "\tv_cmp_eq_u32 s[58:59], 63, v26\n"
        "\ts_lshl_b32 s57, s40, 1\n"
        "\ts_add_u32 s57, s57, s62\n"
        "\ts_lshl_b32 s57, s57, 8\n"
        "\tv_cndmask_b32 v242, v66, v242, vcc\n"
        "\tv_cndmask_b32 v23, v66, 0, s[58:59]\n"
        "\tv_and_b32 v64, 0x7f, v0\n"
        "\tv_lshl_add_u32 v69, v64, 1, s57\n"
        "\tv_mov_b32 v70, 0\n"
        "\tv_mov_b32 v71, 0\n"
        "\tv_mov_b32 v72, 0\n"
        "\tv_mov_b32 v73, 0\n"
        "\tv_mov_b32 v74, 0\n"
        "\tv_mov_b32 v75, 0\n"
        "\tv_mov_b32 v216, -1\n"
        "\tv_mov_b32 v217, -1\n"
        "\tv_mov_b32 v218, -1\n"
        "\tv_mov_b32 v219, -1\n"
        "\ts_lshl_b32 s63, s40, 2\n"
        "\ts_lshl_b32 s58, s56, 2\n"
        "\ts_add_u32 s58, s58, s3\n"
        "\ts_lshl_b32 s58, s58, 9\n"
        "\tv_lshlrev_b32 v80, 7, v26\n"
        "\tv_cmp_gt_u32 vcc, 4, v26\n"
        "\ts_getpc_b64 s[76:77]\n"
        "LPC:\n"
        "\tv_add_u32 v80, s58, v80\n"
        "\ts_mov_b32 s78, LEND-LPC\n"
        "\ts_mov_b32 s79, 0x20000\n"
        "\tv_cndmask_b32 v80, v66, v80, vcc\n"
        "\ts_lshl_b32 s57, s2, 2\n"
        "\ts_add_u32 s57, s57, s3\n"
        "\ts_lshr_b32 s58, s57, 6\n"
        "\ts_mul_i32 s58, s58, 48\n"
        "\ts_add_u32 s58, s58, s57\n"
        "\ts_lshl_b32 s58, s58, 10\n"
        "\tv_cmp_gt_u32 vcc, 8, v26\n"
        "\tv_lshlrev_b32 v64, 7, v26\n"
        "\ts_mul_i32 s59, s57, 1366\n"
        "\tv_cndmask_b32 v64, v66, v64, vcc\n"
        "\ts_lshr_b32 s59, s59, 16\n"
        "\ts_add_u32 s59, s59, 1\n"
        "\ts_lshl_b32 s59, s59, 6\n"
        "\ts_add_u32 s59, s59, s57\n"
        "\ts_lshl_b32 s59, s59, 10\n"
        "\tv_add_u32 v32, s58, v64\n"
        "\tv_add_u32 v33, s59, v64\n"
        "\ts_lshr_b32 s57, s40, 4\n"
        "\ts_mul_i32 s57, s57, 114688\n"
        "\ts_and_b32 s58, s40, 15\n"
        "\ts_mul_i32 s58, s58, 224\n"
        "\ts_add_u32 s67, s57, s58\n"
        "\ts_lshl_b32 s57, s3, 7\n"
        "\tv_add_u32 v63, s57, v26\n"
        "\tv_mul_u32_u24 v64, 4682, v63\n"
        "\tv_lshrrev_b32 v64, 16, v64\n"
        "\tv_mul_u32_u24 v64, 3360, v64\n"
        "\tv_lshl_add_u32 v34, v63, 4, v64\n"
        "\tv_add_u32 v63, s57, v26\n"
        "\tv_add_u32 v63, 64, v63\n"
        "\tv_mul_u32_u24 v64, 4682, v63\n"
        "\tv_lshrrev_b32 v64, 16, v64\n"
        "\tv_mul_u32_u24 v64, 3360, v64\n"
        "\tv_lshl_add_u32 v35, v63, 4, v64\n"
        "\tv_cmp_gt_u32 vcc, 448, v63\n"
        "\ts_lshl_b32 s57, s3, 11\n"
        "\ts_add_u32 s94, s57, 102400\n"
        "\tv_cndmask_b32 v35, v66, v35, vcc\n"
        "\ts_waitcnt lgkmcnt(0)\n"
        "\tbuffer_load_dwordx4 v[84:87], v1, s[4:7], s45 offen\n"
        "\tbuffer_load_dwordx4 v[88:91], v1, s[4:7], s45 offen offset:1024\n"
        "\tbuffer_load_dwordx2 v[82:83], off, s[8:11], 0 sc1\n"
        "\ts_load_dword s81, s[36:37], s63\n"
        "\ts_load_dword s82, s[38:39], s63\n"
        "\ts_mov_b32 s83, 0xffff0000\n"
        "\ts_mov_b32 s84, 0x05040100\n"
        "\tglobal_load_short_d16_hi v70, v69, s[24:25]\n"
        "\ts_mov_b32 s85, 0x07060302\n"
        "\ts_mov_b32 s88, 0x3f80\n"
        "\ts_mov_b32 s89, 0x3f800000\n"
        "\ts_mov_b32 s90, 0x3f803f80\n"
        "\tglobal_load_short_d16_hi v71, v69, s[26:27]\n"
        "\ts_mov_b32 s86, 0x9e3779b1\n"
        "\ts_mov_b32 s87, 0x85ebca6b\n"
        "\ts_mov_b32 s54, 0x4000\n"
        "\ts_lshr_b32 s56, s2, 3\n"
        "\tglobal_load_short_d16_hi v72, v69, s[28:29]\n"
        "\ts_lshr_b32 s57, s56, 3\n"
        "\ts_lshr_b32 s58, s55, 1\n"
        "\ts_lshl_b32 s58, s58, 2\n"
        "\ts_add_u32 s41, s57, s58\n"
        "\tglobal_load_short_d16_hi v73, v69, s[30:31]\n"
        "\ts_and_b32 s44, s56, 7\n"
        "\ts_and_b32 s58, s55, 1\n"
        "\ts_lshl_b32 s59, s58, 3\n"
        "\ts_add_u32 s42, s59, s44\n"
        "\tglobal_load_short_d16_hi v74, v69, s[32:33]\n"
        "\ts_lshl_b32 s59, s41, 1\n"
        "\ts_lshl_b32 s58, s58, 2\n"
        "\ts_and_b32 s57, s56, 3\n"
        "\ts_add_u32 s44, s57, s58\n"
        "\tglobal_load_short_d16_hi v75, v69, s[34:35]\n"
        "\ts_bfe_u32 s58, s56, 0x10002\n"
        "\ts_add_u32 s43, s59, s58\n"
        "\ts_mul_i32 s64, s3, 458752\n"
        "\ts_mul_i32 s66, s3, 28672\n"
        "\tbuffer_load_dword v92, v67, s[4:7], s46 offen offset:0\n"
        "\tv_and_b32 v27, 15, v0\n"
        "\tv_lshrrev_b32 v28, 4, v26\n"
        "\tv_lshlrev_b32 v24, 4, v0\n"
        "\tv_and_b32 v63, 7, v0\n"
        "\tbuffer_load_dword v93, v67, s[4:7], s46 offen offset:1024\n"
        "\tv_lshlrev_b32 v64, 1, v28\n"
        "\tv_xor_b32 v63, v63, v64\n"
        "\tv_lshlrev_b32 v63, 4, v63\n"
        "\tv_lshrrev_b32 v64, 3, v26\n"
        "\tbuffer_load_dword v94, v67, s[4:7], s46 offen offset:2048\n"
        "\tv_lshl_add_u32 v63, v64, 10, v63\n"
        "\ts_lshl_b32 s57, s44, 7\n"
        "\tv_add_u32 v244, s57, v63\n"
        "\tv_add_u32 v245, 7168, v244\n"
        "\tbuffer_load_dword v95, v67, s[4:7], s46 offen offset:3072\n"
        "\tv_add_u32 v246, 14336, v244\n"
        "\tv_add_u32 v247, 21504, v244\n"
        "\tv_and_b32 v63, 3, v64\n"
        "\tv_lshl_add_u32 v64, v29, 7, v30\n"
        "\tbuffer_load_dword v96, v68, s[4:7], s46 offen offset:0\n"
        "\tv_add_u32 v64, s66, v64\n"
        "\tv_xor_b32 v65, 0, v63\n"
        "\tv_lshl_add_u32 v248, v65, 5, v64\n"
        "\tv_xor_b32 v65, 1, v63\n"
        "\tv_lshl_add_u32 v249, v65, 5, v64\n"
        "\tv_xor_b32 v65, 2, v63\n"
        "\tv_lshl_add_u32 v250, v65, 5, v64\n"
        "\tv_xor_b32 v65, 3, v63\n"
        "\tv_lshl_add_u32 v251, v65, 5, v64\n"
        "\tbuffer_load_dword v97, v68, s[4:7], s46 offen offset:1024\n"
        "\tv_and_b32 v63, 3, v0\n"
        "\ts_mov_b32 s57, 0x3C6EF372\n"
        "\tv_mul_lo_u32 v64, v63, s57\n"
        "\tv_add_u32 v220, 0x9e3779b1, v64\n"
        "\tv_add_u32 v221, 0x59f0e927, v64\n"
        "\tv_add_u32 v222, 0x143f50c5, v64\n"
        "\tv_add_u32 v223, 0xcd22b08b, v64\n"
        "\tv_add_u32 v224, 0x849b0879, v64\n"
        "\tv_add_u32 v225, 0x3aa8588f, v64\n"
        "\tbuffer_load_dword v98, v68, s[4:7], s46 offen offset:2048\n"
        "\tv_add_u32 v226, 0xef4aa0cd, v64\n"
        "\tv_add_u32 v227, 0xa281e133, v64\n"
        "\tv_and_b32 v64, 0x7f, v0\n"
        "\tv_lshlrev_b32 v64, 2, v64\n"
        "\ts_mul_i32 s57, s62, 20992\n"
        "\ts_add_u32 s57, s57, 114688\n"
        "\ts_mul_i32 s58, s62, 57344\n"
        "\ts_add_u32 s58, s58, 24576\n"
        "\tv_add_u32 v184, s57, v64\n"
        "\tbuffer_load_dword v99, v68, s[4:7], s46 offen offset:3072\n"
        "\tv_add_u32 v192, s58, v64\n"
        "\tv_xor_b32 v65, 32, v64\n"
        "\tv_add_u32 v185, s57, v65\n"
        "\tv_add_u32 v193, s58, v65\n"
        "\tv_xor_b32 v65, 64, v64\n"
        "\tv_add_u32 v186, s57, v65\n"
        "\tv_add_u32 v194, s58, v65\n"
        "\tv_xor_b32 v65, 96, v64\n"
        "\tv_add_u32 v187, s57, v65\n"
        "\tbuffer_load_dword a254, v80, s[76:79], 0 offen\n"
        "\tv_add_u32 v195, s58, v65\n"
        "\tv_xor_b32 v65, 128, v64\n"
        "\tv_add_u32 v188, s57, v65\n"
        "\tv_add_u32 v196, s58, v65\n"
        "\tv_xor_b32 v65, 160, v64\n"
        "\tv_add_u32 v189, s57, v65\n"
        "\tv_add_u32 v197, s58, v65\n"
        "\tv_xor_b32 v65, 192, v64\n"
        "\tv_add_u32 v190, s57, v65\n"
        "\ts_mov_b32 m0, s94\n"
        "\ts_nop 0\n"
        "\tbuffer_load_dwordx4 v34, s[16:19], s67 offen lds\n"
        "\tv_add_u32 v198, s58, v65\n"
        "\tv_xor_b32 v65, 224, v64\n"
        "\tv_add_u32 v191, s57, v65\n"
        "\tv_add_u32 v199, s58, v65\n"
        "\tv_and_b32 v63, 7, v0\n"
        "\tv_lshlrev_b32 v65, 1, v63\n"
        "\tv_xor_b32 v65, v65, v28\n"
        "\tv_lshlrev_b32 v65, 4, v65\n"
        "\tv_lshl_add_u32 v66, v63, 9, v65\n"
        "\ts_add_u32 m0, s94, 1024\n"
        "\ts_nop 0\n"
        "\tbuffer_load_dwordx4 v35, s[16:19], s67 offen lds\n"
        "\tv_lshrrev_b32 v63, 3, v27\n"
        "\tv_mul_u32_u24 v63, 28672, v63\n"
        "\tv_add3_u32 v202, v66, v63, s58\n"
        "\ts_and_b32 s59, s3, 1\n"
        "\ts_cmp_eq_u32 s59, 0\n"
        "\ts_cselect_b64 s[58:59], -1, 0\n"
        "\ts_cselect_b32 s60, 0, 12800\n"
        "\ts_cselect_b32 s61, 8192, s60\n"
        "\ts_cselect_b32 s96, 0, 25\n"
        "\ts_cselect_b32 s97, 16, 25\n"
        "\tbuffer_load_dword a252, v32, s[12:15], 0 offen\n"
        "\ts_add_u32 s60, s60, s57\n"
        "\ts_add_u32 s61, s61, s57\n"
        "\tv_add_u32 v63, s96, v27\n"
        "\tv_and_b32 v63, 7, v63\n"
        "\tv_lshlrev_b32 v63, 1, v63\n"
        "\tv_xor_b32 v63, v63, v28\n"
        "\tv_lshlrev_b32 v63, 4, v63\n"
        "\tv_lshl_add_u32 v63, v27, 9, v63\n"
        "\tv_add_u32 v200, s60, v63\n"
        "\tv_add_u32 v63, s97, v27\n"
        "\tv_and_b32 v63, 7, v63\n"
        "\tv_lshlrev_b32 v63, 1, v63\n"
        "\tv_xor_b32 v63, v63, v28\n"
        "\tv_lshlrev_b32 v63, 4, v63\n"
        "\tv_lshl_add_u32 v63, v27, 9, v63\n"
        "\tv_add_u32 v201, s61, v63\n"
        "\tv_and_b32 v63, 1, v28\n"
        "\tv_lshrrev_b32 v64, 1, v28\n"
        "\tv_lshlrev_b32 v100, 3, v27\n"
        "\tv_lshl_add_u32 v100, v63, 2, v100\n"
        "\tv_lshl_add_u32 v100, v64, 7, v100\n"
        "\tv_cmp_gt_u32 vcc, 2, v28\n"
        "\tv_lshlrev_b32 v103, 1, v27\n"
        "\ts_lshl_b32 s60, s62, 1\n"
        "\tv_cndmask_b32 v103, v27, v103, vcc\n"
        "\tv_lshl_add_u32 v103, v28, 6, v103\n"
        "\tv_add_u32 v103, 384, v103\n"
        "\tv_cndmask_b32 v100, v103, v100, s[58:59]\n"
        "\tv_cndmask_b32 v101, 16, 1, vcc\n"
        "\tv_cndmask_b32 v101, v101, 1, s[58:59]\n"
        "\tv_cndmask_b32 v102, 32, 2, s[58:59]\n"
        "\ts_add_u32 s60, s60, 158736\n"
        "\tv_lshl_add_u32 v204, v100, 2, s60\n"
        "\tv_lshl_add_u32 v205, v101, 2, v204\n"
        "\tv_lshl_add_u32 v206, v102, 2, v204\n"
        "\tv_lshl_add_u32 v207, v102, 2, v205\n"
        "\tv_lshlrev_b32 v103, 2, v27\n"
        "\tv_lshl_add_u32 v103, v28, 6, v103\n"
        "\ts_add_u32 s61, s60, 1024\n"
        "\tv_lshl_add_u32 v103, v103, 2, s61\n"
        "\tv_mov_b32 v101, 163072\n"
        "\ts_and_b64 vcc, vcc, s[58:59]\n"
        "\tv_cndmask_b32 v208, v101, v103, vcc\n"
        "\tv_add_u32 v209, 4, v208\n"
        "\tv_add_u32 v210, 8, v208\n"
        "\tv_add_u32 v211, 12, v208\n"
        "\tv_cmp_eq_u32 vcc, 32, v26\n"
        "\ts_add_u32 s60, s60, 2560\n"
        "\ts_and_b64 vcc, vcc, s[58:59]\n"
        "\tv_mov_b32 v102, s60\n"
        "\tv_cndmask_b32 v208, v208, v102, vcc\n"
        "\tv_mov_b32 v104, 0\n"
        "\tv_mov_b32 v105, 0\n"
        "\tv_mov_b32 v106, 0\n"
        "\tv_mov_b32 v107, 0\n"
        "\tv_mov_b32 v102, 158720\n"
        "\tds_write_b128 v102, v[104:107]\n"
        "\tv_lshlrev_b32 v64, 3, v0\n"
        "\tv_sub_u32 v63, 2037, v64\n"
        "\tv_cvt_f32_i32 v63, v63\n"
        "\tv_sub_u32 v64, 19061, v64\n"
        "\ts_movk_i32 s57, 17152\n"
        "\ts_mov_b32 s60, 90640\n"
        "\tv_add_f32 v229, 0x0, v63\n"
        "\tv_max_f32 v229, 0, v229\n"
        "\tv_cvt_pk_bf16_f32 v229, v229, 0\n"
        "\tv_add_u32 v65, 0, v64\n"
        "\tv_med3_i32 v229, v229, v65, s57\n"
        "\tv_lshl_add_u32 v229, v229, 2, s60\n"
        "\tv_add_f32 v230, 0x3f800000, v63\n"
        "\tv_max_f32 v230, 0, v230\n"
        "\tv_cvt_pk_bf16_f32 v230, v230, 0\n"
        "\tv_add_u32 v65, 1, v64\n"
        "\tv_med3_i32 v230, v230, v65, s57\n"
        "\tv_lshl_add_u32 v230, v230, 2, s60\n"
        "\tv_add_f32 v231, 0x40000000, v63\n"
        "\tv_max_f32 v231, 0, v231\n"
        "\tv_cvt_pk_bf16_f32 v231, v231, 0\n"
        "\tv_add_u32 v65, 2, v64\n"
        "\tv_med3_i32 v231, v231, v65, s57\n"
        "\tv_lshl_add_u32 v231, v231, 2, s60\n"
        "\tv_add_f32 v232, 0x40400000, v63\n"
        "\tv_cvt_pk_bf16_f32 v232, v232, 0\n"
        "\tv_add_u32 v65, 3, v64\n"
        "\tv_med3_i32 v232, v232, v65, s57\n"
        "\tv_lshl_add_u32 v232, v232, 2, s60\n"
        "\tv_add_f32 v233, 0x40800000, v63\n"
        "\tv_cvt_pk_bf16_f32 v233, v233, 0\n"
        "\tv_add_u32 v65, 4, v64\n"
        "\tv_med3_i32 v233, v233, v65, s57\n"
        "\tv_lshl_add_u32 v233, v233, 2, s60\n"
        "\tv_add_f32 v234, 0x40a00000, v63\n"
        "\tv_cvt_pk_bf16_f32 v234, v234, 0\n"
        "\tv_add_u32 v65, 5, v64\n"
        "\tv_med3_i32 v234, v234, v65, s57\n"
        "\tv_lshl_add_u32 v234, v234, 2, s60\n"
        "\tv_add_f32 v235, 0x40c00000, v63\n"
        "\tv_cvt_pk_bf16_f32 v235, v235, 0\n"
        "\tv_add_u32 v65, 6, v64\n"
        "\tv_med3_i32 v235, v235, v65, s57\n"
        "\tv_lshl_add_u32 v235, v235, 2, s60\n"
        "\tv_add_f32 v236, 0x40e00000, v63\n"
        "\tv_cvt_pk_bf16_f32 v236, v236, 0\n"
        "\tv_add_u32 v65, 7, v64\n"
        "\tv_med3_i32 v236, v236, v65, s57\n"
        "\tv_lshl_add_u32 v236, v236, 2, s60\n"
        "\tv_add_f32 v237, 0x41000000, v63\n"
        "\tv_cvt_pk_bf16_f32 v237, v237, 0\n"
        "\tv_add_u32 v65, 8, v64\n"
        "\tv_med3_i32 v237, v237, v65, s57\n"
        "\tv_lshl_add_u32 v237, v237, 2, s60\n"
        "\tv_add_f32 v238, 0x41100000, v63\n"
        "\tv_cvt_pk_bf16_f32 v238, v238, 0\n"
        "\tv_add_u32 v65, 9, v64\n"
        "\tv_med3_i32 v238, v238, v65, s57\n"
        "\tv_lshl_add_u32 v238, v238, 2, s60\n"
        "\tv_add_f32 v239, 0x41200000, v63\n"
        "\tv_cvt_pk_bf16_f32 v239, v239, 0\n"
        "\tv_add_u32 v65, 10, v64\n"
        "\tv_med3_i32 v239, v239, v65, s57\n"
        "\tv_lshl_add_u32 v239, v239, 2, s60\n"
        "\ts_lshl_b32 s57, s41, 2\n"
        "\ts_add_u32 s57, s57, s3\n"
        "\ts_mul_i32 s58, s57, 33536\n"
        "\ts_add_u32 s73, s58, 0x2c0000\n"
        "\ts_lshl_b32 s58, s57, 4\n"
        "\ts_add_u32 s58, s58, s42\n"
        "\ts_lshl_b32 s58, s58, 10\n"
        "\ts_add_u32 s72, s58, 0xb00000\n"
        "\ts_lshl_b32 s58, s57, 5\n"
        "\ts_add_u32 s58, s58, s42\n"
        "\ts_mul_i32 s58, s58, 3072\n"
        "\ts_add_u32 s70, s58, 0x500000\n"
        "\ts_add_u32 s71, s70, 49152\n"
        "\ts_mul_i32 s57, s40, 8704\n"
        "\ts_add_u32 s68, s57, 0x40000\n"
        "\ts_lshl_b32 s57, s40, 3\n"
        "\ts_add_u32 s69, s57, 0x8000\n"
        "\ts_mov_b32 s97, 4096\n"
        "\ts_mov_b32 s98, 8192\n"
        "\ts_mov_b32 s99, 12288\n"
        "\ts_mov_b32 s63, 16384\n"
        "\ts_add_u32 s65, s68, 4352\n"
        "\ts_or_b32 s57, s2, s3\n"
        "\ts_cmp_eq_u32 s57, 0\n"
        "\ts_cselect_b32 s57, 0, 0x40000000\n"
        "\tv_or_b32 v18, s57, v23\n"
        "\ts_cmp_eq_u32 s3, 0\n"
        "\ts_cselect_b32 s57, 0, 0x40000000\n"
        "\tv_or_b32 v19, s57, v23\n"
        "\ts_lshl_b32 s57, s2, 3\n"
        "\ts_add_u32 s61, s57, 24576\n"
        "\tv_mul_u32_u24 v64, 224, v29\n"
        "\ts_lshl_b32 s57, s3, 5\n"
        "\ts_add_u32 s57, s57, 102400\n"
        "\tv_add3_u32 v36, v64, v30, s57\n"
        "\ts_and_b32 s57, s40, 15\n"
        "\ts_lshr_b32 s58, s40, 4\n"
        "\ts_mul_i32 s59, s57, 112\n"
        "\ts_mul_i32 s60, s58, 7\n"
        "\ts_add_u32 s59, s59, s60\n"
        "\ts_add_u32 s59, s59, s3\n"
        "\ts_lshl_b32 s59, s59, 10\n"
        "\ts_add_u32 s29, s59, 0xc00000\n"
        "\ts_mul_i32 s57, s57, 7\n"
        "\ts_add_u32 s57, s57, s3\n"
        "\ts_lshl_b32 s57, s57, 10\n"
        "\ts_lshr_b32 s59, s58, 2\n"
        "\ts_lshl_b32 s59, s59, 8\n"
        "\ts_add_u32 s57, s57, s59\n"
        "\ts_and_b32 s58, s58, 3\n"
        "\ts_lshl_b32 s58, s58, 2\n"
        "\ts_add_u32 s57, s57, s58\n"
        "\ts_add_u32 s30, s57, 0xdc0000\n"
        "\tv_mov_b32 v65, 0x40000000\n"
        "\tv_cmp_gt_u32 vcc, 16, v26\n"
        "\ts_cmp_eq_u32 s3, 3\n"
        "\ts_cselect_b32 s57, 0x40000000, 0\n"
        "\tv_cndmask_b32 v37, v65, v1, vcc\n"
        "\tv_or_b32 v38, s57, v1\n"
        "\tv_or_b32 v39, s57, v37\n"
        "\ts_mul_i32 s57, s42, 112\n"
        "\ts_mul_i32 s58, s3, 28\n"
        "\ts_add_u32 s57, s57, s58\n"
        "\ts_lshl_b32 s57, s57, 10\n"
        "\ts_add_u32 s80, s57, 0xc00000\n"
        "\ts_lshl_b32 s57, s2, 3\n"
        "\ts_add_u32 s75, s57, 28672\n"
        "\tv_mov_b32 v63, s90\n"
        "\tv_accvgpr_write_b32 a224, v63\n"
        "\tv_accvgpr_write_b32 a225, v63\n"
        "\tv_accvgpr_write_b32 a226, v63\n"
        "\tv_accvgpr_write_b32 a227, v63\n"
        "\ts_waitcnt vmcnt(20)\n"
        "\tv_dot2_f32_bf16 v100, v84, s90, 0\n"
        "\tv_dot2_f32_bf16 v101, v84, v84, 0\n"
        "\ts_waitcnt vmcnt(19)\n"
        "\tv_dot2_f32_bf16 v102, v88, s90, 0\n"
        "\tv_dot2_f32_bf16 v103, v88, v88, 0\n"
        "\tv_dot2_f32_bf16 v100, v85, s90, v100\n"
        "\tv_dot2_f32_bf16 v101, v85, v85, v101\n"
        "\tv_dot2_f32_bf16 v102, v89, s90, v102\n"
        "\tv_dot2_f32_bf16 v103, v89, v89, v103\n"
        "\tv_dot2_f32_bf16 v100, v86, s90, v100\n"
        "\tv_dot2_f32_bf16 v101, v86, v86, v101\n"
        "\tv_dot2_f32_bf16 v102, v90, s90, v102\n"
        "\tv_dot2_f32_bf16 v103, v90, v90, v103\n"
        "\tv_dot2_f32_bf16 v100, v87, s90, v100\n"
        "\tv_dot2_f32_bf16 v101, v87, v87, v101\n"
        "\tv_dot2_f32_bf16 v102, v91, s90, v102\n"
        "\tv_dot2_f32_bf16 v103, v91, v91, v103\n"
        "\ts_waitcnt vmcnt(18)\n"
        "\tv_readfirstlane_b32 s52, v82\n"
        "\tv_readfirstlane_b32 s53, v83\n"
        "\ts_mov_b32 s57, 0x3b000000\n"
        "\tv_add_f32_dpp v100, v100, v100 row_shr:1 row_mask:0xf bank_mask:0xf bound_ctrl:1\n"
        "\tv_add_f32_dpp v101, v101, v101 row_shr:1 row_mask:0xf bank_mask:0xf bound_ctrl:1\n"
        "\tv_add_f32_dpp v102, v102, v102 row_shr:1 row_mask:0xf bank_mask:0xf bound_ctrl:1\n"
        "\tv_add_f32_dpp v103, v103, v103 row_shr:1 row_mask:0xf bank_mask:0xf bound_ctrl:1\n"
        "\tv_add_f32_dpp v100, v100, v100 row_shr:2 row_mask:0xf bank_mask:0xf bound_ctrl:1\n"
        "\tv_add_f32_dpp v101, v101, v101 row_shr:2 row_mask:0xf bank_mask:0xf bound_ctrl:1\n"
        "\tv_add_f32_dpp v102, v102, v102 row_shr:2 row_mask:0xf bank_mask:0xf bound_ctrl:1\n"
        "\tv_add_f32_dpp v103, v103, v103 row_shr:2 row_mask:0xf bank_mask:0xf bound_ctrl:1\n"
        "\tv_add_f32_dpp v100, v100, v100 row_shr:4 row_mask:0xf bank_mask:0xf bound_ctrl:1\n"
        "\tv_add_f32_dpp v101, v101, v101 row_shr:4 row_mask:0xf bank_mask:0xf bound_ctrl:1\n"
        "\tv_add_f32_dpp v102, v102, v102 row_shr:4 row_mask:0xf bank_mask:0xf bound_ctrl:1\n"
        "\tv_add_f32_dpp v103, v103, v103 row_shr:4 row_mask:0xf bank_mask:0xf bound_ctrl:1\n"
        "\tv_add_f32_dpp v100, v100, v100 row_shr:8 row_mask:0xf bank_mask:0xf bound_ctrl:1\n"
        "\tv_add_f32_dpp v101, v101, v101 row_shr:8 row_mask:0xf bank_mask:0xf bound_ctrl:1\n"
        "\tv_add_f32_dpp v102, v102, v102 row_shr:8 row_mask:0xf bank_mask:0xf bound_ctrl:1\n"
        "\tv_add_f32_dpp v103, v103, v103 row_shr:8 row_mask:0xf bank_mask:0xf bound_ctrl:1\n"
        "\tv_add_f32_dpp v100, v100, v100 row_bcast:15 row_mask:0xa bank_mask:0xf\n"
        "\tv_add_f32_dpp v101, v101, v101 row_bcast:15 row_mask:0xa bank_mask:0xf\n"
        "\tv_add_f32_dpp v102, v102, v102 row_bcast:15 row_mask:0xa bank_mask:0xf\n"
        "\tv_add_f32_dpp v103, v103, v103 row_bcast:15 row_mask:0xa bank_mask:0xf\n"
        "\tv_add_f32_dpp v100, v100, v100 row_bcast:31 row_mask:0xc bank_mask:0xf\n"
        "\tv_add_f32_dpp v101, v101, v101 row_bcast:31 row_mask:0xc bank_mask:0xf\n"
        "\tv_add_f32_dpp v102, v102, v102 row_bcast:31 row_mask:0xc bank_mask:0xf\n"
        "\tv_add_f32_dpp v103, v103, v103 row_bcast:31 row_mask:0xc bank_mask:0xf\n"
        "\ts_andn2_b32 s28, 0x80, s52\n"
        "\ts_add_u32 s52, s52, 0x7F4A7C15\n"
        "\ts_addc_u32 s53, s53, 0x9E3779B9\n"
        "\ts_andn2_b32 s52, s52, 0x80\n"
        "\ts_or_b32 s52, s52, s28\n"
        "\tv_mul_f32 v100, s57, v100\n"
        "\tv_mul_f32 v101, s57, v101\n"
        "\tv_mul_f32 v102, s57, v102\n"
        "\tv_mul_f32 v103, s57, v103\n"
        "\tv_mov_b32 v20, s52\n"
        "\tv_mov_b32 v21, s53\n"
        "\tv_fma_f32 v101, -v100, v100, v101\n"
        "\tv_fma_f32 v103, -v102, v102, v103\n"
        "\tv_max_f32 v101, 0, v101\n"
        "\tv_max_f32 v103, 0, v103\n"
        "\tv_cvt_pk_bf16_f32 v104, v101, v103\n"
        "\tv_lshlrev_b32 v101, 16, v104\n"
        "\tv_and_b32 v103, s83, v104\n"
        "\tv_add_f32 v101, 0x3727c5ac, v101\n"
        "\tv_add_f32 v103, 0x3727c5ac, v103\n"
        "\tv_cvt_pk_bf16_f32 v104, v101, v103\n"
        "\tv_lshlrev_b32 v101, 16, v104\n"
        "\tv_and_b32 v103, s83, v104\n"
        "\tv_sqrt_f32 v101, v101\n"
        "\tv_sqrt_f32 v103, v103\n"
        "\tv_cvt_pk_bf16_f32 v108, v100, v101\n"
        "\tv_cvt_pk_bf16_f32 v109, v102, v103\n"
        "\tv_mad_u64_u32 v[110:111], s[94:95], v108, s86, v[20:21]\n"
        "\tv_mad_u64_u32 v[110:111], s[94:95], v109, s87, v[110:111]\n"
        "\tbuffer_store_dwordx4 v[108:111], v23, s[8:11], s50 offen sc1\n"
        "\ts_waitcnt vmcnt(11)\n"
        "\tv_perm_b32 v112, v93, v92, s84\n"
        "\ts_waitcnt vmcnt(9)\n"
        "\tv_perm_b32 v113, v95, v94, s84\n"
        "\ts_waitcnt vmcnt(7)\n"
        "\tv_perm_b32 v114, v97, v96, s84\n"
        "\ts_waitcnt vmcnt(5)\n"
        "\tv_perm_b32 v115, v99, v98, s84\n"
        "\tbuffer_store_dwordx4 v[112:115], v241, s[8:11], s48 offen sc1\n"
        "\tv_perm_b32 v116, v93, v92, s85\n"
        "\tv_perm_b32 v117, v95, v94, s85\n"
        "\tv_perm_b32 v118, v97, v96, s85\n"
        "\tv_perm_b32 v119, v99, v98, s85\n"
        "\tbuffer_store_dwordx4 v[116:119], v241, s[8:11], s51 offen sc1\n"
        "\tv_mad_u64_u32 v[120:121], s[94:95], v112, v220, 0\n"
        "\tv_mad_u64_u32 v[120:121], s[94:95], v113, v221, v[120:121]\n"
        "\tv_mad_u64_u32 v[120:121], s[94:95], v114, v222, v[120:121]\n"
        "\tv_mad_u64_u32 v[120:121], s[94:95], v115, v223, v[120:121]\n"
        "\tv_mad_u64_u32 v[120:121], s[94:95], v116, v224, v[120:121]\n"
        "\tv_mad_u64_u32 v[120:121], s[94:95], v117, v225, v[120:121]\n"
        "\tv_mad_u64_u32 v[120:121], s[94:95], v118, v226, v[120:121]\n"
        "\tv_mad_u64_u32 v[120:121], s[94:95], v119, v227, v[120:121]\n"
        "\ts_nop 1\n"
        "\tv_mov_b32_dpp v122, v120 quad_perm:[1,0,3,2] row_mask:0xf bank_mask:0xf\n"
        "\tv_mov_b32_dpp v123, v121 quad_perm:[1,0,3,2] row_mask:0xf bank_mask:0xf\n"
        "\tv_lshl_add_u64 v[120:121], v[120:121], 0, v[122:123]\n"
        "\ts_nop 1\n"
        "\tv_mov_b32_dpp v122, v120 quad_perm:[2,3,0,1] row_mask:0xf bank_mask:0xf\n"
        "\tv_mov_b32_dpp v123, v121 quad_perm:[2,3,0,1] row_mask:0xf bank_mask:0xf\n"
        "\tv_lshl_add_u64 v[120:121], v[120:121], 0, v[122:123]\n"
        "\tv_lshl_add_u64 v[120:121], v[120:121], 0, v[20:21]\n"
        "\tbuffer_store_dwordx2 v[120:121], v242, s[8:11], s49 offen sc1\n"
        "\ts_lshl_b32 s57, s62, 4\n"
        "\ts_waitcnt lgkmcnt(0)\n"
        "\ts_lshr_b32 s57, s81, s57\n"
        "\ts_lshl_b32 s57, s57, 16\n"
        "\tv_mov_b32 v140, 0x3fb8aa3b\n"
        "\tv_mul_f32 v124, s57, v140\n"
        "\tv_exp_f32 v124, v124\n"
        "\ts_nop 0\n"
        "\tv_cvt_pk_bf16_f32 v124, v124, v124\n"
        "\tv_lshlrev_b32 v124, 16, v124\n"
        "\tv_mul_f32 v125, v124, v70\n"
        "\tv_mul_f32 v126, v124, v71\n"
        "\tv_mul_f32 v125, v140, v125\n"
        "\tv_mul_f32 v126, 0.15915494, v126\n"
        "\tv_mul_f32 v142, v124, v72\n"
        "\tv_mul_f32 v143, v124, v73\n"
        "\tv_pk_mul_f32 v[140:141], v[74:75], v[142:143] op_sel_hi:[0,1]\n"
        "\tv_pk_fma_f32 v[128:129], v[74:75], v[142:143], v[140:141] op_sel:[1,1,0] op_sel_hi:[1,0,1] neg_lo:[1,0,0]\n"
        "\tv_mul_f32 v144, 0x41000000, v125\n"
        "\tv_mul_f32 v145, 0x41000000, v126\n"
        "\tv_fract_f32 v145, v145\n"
        "\tv_exp_f32 v144, v144\n"
        "\tv_cos_f32 v136, v145\n"
        "\tv_sin_f32 v137, v145\n"
        "\ts_nop 0\n"
        "\tv_pk_mul_f32 v[136:137], v[136:137], v[144:145] op_sel_hi:[1,0]\n"
        "\tv_fract_f32 v145, v126\n"
        "\tv_exp_f32 v144, v125\n"
        "\tv_cos_f32 v130, v145\n"
        "\tv_sin_f32 v131, v145\n"
        "\ts_nop 0\n"
        "\tv_pk_mul_f32 v[130:131], v[130:131], v[144:145] op_sel_hi:[1,0]\n"
        "\tv_mul_f32 v144, 0x43000000, v125\n"
        "\tv_mul_f32 v145, 0x43000000, v126\n"
        "\tv_fract_f32 v145, v145\n"
        "\tv_exp_f32 v144, v144\n"
        "\tv_cos_f32 v138, v145\n"
        "\tv_sin_f32 v139, v145\n"
        "\ts_nop 0\n"
        "\tv_pk_mul_f32 v[138:139], v[138:139], v[144:145] op_sel_hi:[1,0]\n"
        "\tv_pk_mul_f32 v[140:141], v[130:131], v[130:131] op_sel_hi:[0,1]\n"
        "\tv_pk_fma_f32 v[132:133], v[130:131], v[130:131], v[140:141] op_sel:[1,1,0] op_sel_hi:[1,0,1] neg_lo:[1,0,0]\n"
        "\tv_pk_mul_f32 v[140:141], v[132:133], v[132:133] op_sel_hi:[0,1]\n"
        "\tv_pk_fma_f32 v[134:135], v[132:133], v[132:133], v[140:141] op_sel:[1,1,0] op_sel_hi:[1,0,1] neg_lo:[1,0,0]\n"
        "\tv_mov_b32 v146, 0x3f80\n"
        "\tds_write_b32 v192, v146 offset:0\n"
        "\tv_cvt_pk_bf16_f32 v147, v136, -v137\n"
        "\tds_write_b32 v193, v147 offset:512\n"
        "\tv_pk_mul_f32 v[140:141], v[136:137], v[136:137] op_sel_hi:[0,1]\n"
        "\tv_pk_fma_f32 v[148:149], v[136:137], v[136:137], v[140:141] op_sel:[1,1,0] op_sel_hi:[1,0,1] neg_lo:[1,0,0]\n"
        "\tv_cvt_pk_bf16_f32 v146, v148, -v149\n"
        "\tds_write_b32 v194, v146 offset:1024\n"
        "\tv_pk_mul_f32 v[140:141], v[148:149], v[136:137] op_sel_hi:[0,1]\n"
        "\tv_pk_fma_f32 v[148:149], v[148:149], v[136:137], v[140:141] op_sel:[1,1,0] op_sel_hi:[1,0,1] neg_lo:[1,0,0]\n"
        "\tv_cvt_pk_bf16_f32 v147, v148, -v149\n"
        "\tds_write_b32 v195, v147 offset:1536\n"
        "\tv_pk_mul_f32 v[140:141], v[148:149], v[136:137] op_sel_hi:[0,1]\n"
        "\tv_pk_fma_f32 v[148:149], v[148:149], v[136:137], v[140:141] op_sel:[1,1,0] op_sel_hi:[1,0,1] neg_lo:[1,0,0]\n"
        "\tv_cvt_pk_bf16_f32 v146, v148, -v149\n"
        "\tds_write_b32 v196, v146 offset:2048\n"
        "\tv_pk_mul_f32 v[140:141], v[148:149], v[136:137] op_sel_hi:[0,1]\n"
        "\tv_pk_fma_f32 v[148:149], v[148:149], v[136:137], v[140:141] op_sel:[1,1,0] op_sel_hi:[1,0,1] neg_lo:[1,0,0]\n"
        "\tv_cvt_pk_bf16_f32 v147, v148, -v149\n"
        "\tds_write_b32 v197, v147 offset:2560\n"
        "\tbuffer_store_dwordx4 v[216:219], v25, s[8:11], s47 offen offset:0 sc1\n"
        "\tbuffer_load_dword a253, v33, s[12:15], 0 offen\n"
        "\tv_pk_mul_f32 v[140:141], v[148:149], v[136:137] op_sel_hi:[0,1]\n"
        "\tv_pk_fma_f32 v[148:149], v[148:149], v[136:137], v[140:141] op_sel:[1,1,0] op_sel_hi:[1,0,1] neg_lo:[1,0,0]\n"
        "\tv_cvt_pk_bf16_f32 v146, v148, -v149\n"
        "\tds_write_b32 v198, v146 offset:3072\n"
        "\tv_pk_mul_f32 v[140:141], v[148:149], v[136:137] op_sel_hi:[0,1]\n"
        "\tv_pk_fma_f32 v[148:149], v[148:149], v[136:137], v[140:141] op_sel:[1,1,0] op_sel_hi:[1,0,1] neg_lo:[1,0,0]\n"
        "\tv_cvt_pk_bf16_f32 v147, v148, -v149\n"
        "\tds_write_b32 v199, v147 offset:3584\n"
        "\tv_pk_mul_f32 v[140:141], v[148:149], v[136:137] op_sel_hi:[0,1]\n"
        "\tv_pk_fma_f32 v[148:149], v[148:149], v[136:137], v[140:141] op_sel:[1,1,0] op_sel_hi:[1,0,1] neg_lo:[1,0,0]\n"
        "\tv_cvt_pk_bf16_f32 v146, v148, -v149\n"
        "\tds_write_b32 v192, v146 offset:28672\n"
        "\tv_pk_mul_f32 v[140:141], v[148:149], v[136:137] op_sel_hi:[0,1]\n"
        "\tv_pk_fma_f32 v[148:149], v[148:149], v[136:137], v[140:141] op_sel:[1,1,0] op_sel_hi:[1,0,1] neg_lo:[1,0,0]\n"
        "\tv_cvt_pk_bf16_f32 v147, v148, -v149\n"
        "\tds_write_b32 v193, v147 offset:29184\n"
        "\tv_pk_mul_f32 v[140:141], v[148:149], v[136:137] op_sel_hi:[0,1]\n"
        "\tv_pk_fma_f32 v[148:149], v[148:149], v[136:137], v[140:141] op_sel:[1,1,0] op_sel_hi:[1,0,1] neg_lo:[1,0,0]\n"
        "\tv_cvt_pk_bf16_f32 v146, v148, -v149\n"
        "\tds_write_b32 v194, v146 offset:29696\n"
        "\tv_pk_mul_f32 v[140:141], v[148:149], v[136:137] op_sel_hi:[0,1]\n"
        "\tv_pk_fma_f32 v[148:149], v[148:149], v[136:137], v[140:141] op_sel:[1,1,0] op_sel_hi:[1,0,1] neg_lo:[1,0,0]\n"
        "\tv_cvt_pk_bf16_f32 v147, v148, -v149\n"
        "\tds_write_b32 v195, v147 offset:30208\n"
        "\tv_pk_mul_f32 v[140:141], v[148:149], v[136:137] op_sel_hi:[0,1]\n"
        "\tv_pk_fma_f32 v[148:149], v[148:149], v[136:137], v[140:141] op_sel:[1,1,0] op_sel_hi:[1,0,1] neg_lo:[1,0,0]\n"
        "\tv_cvt_pk_bf16_f32 v146, v148, -v149\n"
        "\tds_write_b32 v196, v146 offset:30720\n"
        "\tv_pk_mul_f32 v[140:141], v[148:149], v[136:137] op_sel_hi:[0,1]\n"
        "\tv_pk_fma_f32 v[148:149], v[148:149], v[136:137], v[140:141] op_sel:[1,1,0] op_sel_hi:[1,0,1] neg_lo:[1,0,0]\n"
        "\tv_cvt_pk_bf16_f32 v147, v148, -v149\n"
        "\tds_write_b32 v197, v147 offset:31232\n"
        "\tv_pk_mul_f32 v[140:141], v[148:149], v[136:137] op_sel_hi:[0,1]\n"
        "\tv_pk_fma_f32 v[148:149], v[148:149], v[136:137], v[140:141] op_sel:[1,1,0] op_sel_hi:[1,0,1] neg_lo:[1,0,0]\n"
        "\tv_cvt_pk_bf16_f32 v146, v148, -v149\n"
        "\tds_write_b32 v198, v146 offset:31744\n"
        "\tv_pk_mul_f32 v[140:141], v[148:149], v[136:137] op_sel_hi:[0,1]\n"
        "\tv_pk_fma_f32 v[148:149], v[148:149], v[136:137], v[140:141] op_sel:[1,1,0] op_sel_hi:[1,0,1] neg_lo:[1,0,0]\n"
        "\tv_cvt_pk_bf16_f32 v147, v148, -v149\n"
        "\tds_write_b32 v199, v147 offset:32256\n"
        "\tbuffer_store_dwordx4 v[216:219], v25, s[8:11], s47 offen offset:64 sc1\n"
        "\tv_cvt_pk_bf16_f32 v146, v128, v129\n"
        "\tds_write_b32 v184, v146 offset:0\n"
        "\tv_pk_mul_f32 v[140:141], v[128:129], v[138:139] op_sel_hi:[0,1]\n"
        "\tv_pk_fma_f32 v[150:151], v[128:129], v[138:139], v[140:141] op_sel:[1,1,0] op_sel_hi:[1,0,1] neg_lo:[1,0,0]\n"
        "\tv_cvt_pk_bf16_f32 v147, v150, v151\n"
        "\tds_write_b32 v184, v147 offset:4096\n"
        "\tv_pk_mul_f32 v[140:141], v[150:151], v[138:139] op_sel_hi:[0,1]\n"
        "\tv_pk_fma_f32 v[152:153], v[150:151], v[138:139], v[140:141] op_sel:[1,1,0] op_sel_hi:[1,0,1] neg_lo:[1,0,0]\n"
        "\tv_pk_mul_f32 v[140:141], v[128:129], v[130:131] op_sel_hi:[0,1]\n"
        "\tv_pk_fma_f32 v[148:149], v[128:129], v[130:131], v[140:141] op_sel:[1,1,0] op_sel_hi:[1,0,1] neg_lo:[1,0,0]\n"
        "\tv_cvt_pk_bf16_f32 v146, v148, v149\n"
        "\tds_write_b32 v185, v146 offset:512\n"
        "\tv_pk_mul_f32 v[140:141], v[148:149], v[138:139] op_sel_hi:[0,1]\n"
        "\tv_pk_fma_f32 v[150:151], v[148:149], v[138:139], v[140:141] op_sel:[1,1,0] op_sel_hi:[1,0,1] neg_lo:[1,0,0]\n"
        "\tv_cvt_pk_bf16_f32 v147, v150, v151\n"
        "\tds_write_b32 v185, v147 offset:4608\n"
        "\tv_pk_mul_f32 v[140:141], v[148:149], v[130:131] op_sel_hi:[0,1]\n"
        "\tv_pk_fma_f32 v[148:149], v[148:149], v[130:131], v[140:141] op_sel:[1,1,0] op_sel_hi:[1,0,1] neg_lo:[1,0,0]\n"
        "\tv_cvt_pk_bf16_f32 v146, v148, v149\n"
        "\tds_write_b32 v186, v146 offset:1024\n"
        "\tv_pk_mul_f32 v[140:141], v[148:149], v[138:139] op_sel_hi:[0,1]\n"
        "\tv_pk_fma_f32 v[150:151], v[148:149], v[138:139], v[140:141] op_sel:[1,1,0] op_sel_hi:[1,0,1] neg_lo:[1,0,0]\n"
        "\tv_cvt_pk_bf16_f32 v147, v150, v151\n"
        "\tds_write_b32 v186, v147 offset:5120\n"
        "\tv_pk_mul_f32 v[140:141], v[148:149], v[130:131] op_sel_hi:[0,1]\n"
        "\tv_pk_fma_f32 v[148:149], v[148:149], v[130:131], v[140:141] op_sel:[1,1,0] op_sel_hi:[1,0,1] neg_lo:[1,0,0]\n"
        "\tv_cvt_pk_bf16_f32 v146, v148, v149\n"
        "\tds_write_b32 v187, v146 offset:1536\n"
        "\tv_pk_mul_f32 v[140:141], v[148:149], v[138:139] op_sel_hi:[0,1]\n"
        "\tv_pk_fma_f32 v[150:151], v[148:149], v[138:139], v[140:141] op_sel:[1,1,0] op_sel_hi:[1,0,1] neg_lo:[1,0,0]\n"
        "\tv_cvt_pk_bf16_f32 v147, v150, v151\n"
        "\tds_write_b32 v187, v147 offset:5632\n"
        "\tv_pk_mul_f32 v[140:141], v[148:149], v[130:131] op_sel_hi:[0,1]\n"
        "\tv_pk_fma_f32 v[148:149], v[148:149], v[130:131], v[140:141] op_sel:[1,1,0] op_sel_hi:[1,0,1] neg_lo:[1,0,0]\n"
        "\tv_cvt_pk_bf16_f32 v146, v148, v149\n"
        "\tds_write_b32 v188, v146 offset:2048\n"
        "\tv_pk_mul_f32 v[140:141], v[148:149], v[138:139] op_sel_hi:[0,1]\n"
        "\tv_pk_fma_f32 v[150:151], v[148:149], v[138:139], v[140:141] op_sel:[1,1,0] op_sel_hi:[1,0,1] neg_lo:[1,0,0]\n"
        "\tv_cvt_pk_bf16_f32 v147, v150, v151\n"
        "\tds_write_b32 v188, v147 offset:6144\n"
        "\tv_pk_mul_f32 v[140:141], v[148:149], v[130:131] op_sel_hi:[0,1]\n"
        "\tv_pk_fma_f32 v[148:149], v[148:149], v[130:131], v[140:141] op_sel:[1,1,0] op_sel_hi:[1,0,1] neg_lo:[1,0,0]\n"
        "\tv_cvt_pk_bf16_f32 v146, v148, v149\n"
        "\tds_write_b32 v189, v146 offset:2560\n"
        "\tv_pk_mul_f32 v[140:141], v[148:149], v[138:139] op_sel_hi:[0,1]\n"
        "\tv_pk_fma_f32 v[150:151], v[148:149], v[138:139], v[140:141] op_sel:[1,1,0] op_sel_hi:[1,0,1] neg_lo:[1,0,0]\n"
        "\tv_cvt_pk_bf16_f32 v147, v150, v151\n"
        "\tds_write_b32 v189, v147 offset:6656\n"
        "\tv_pk_mul_f32 v[140:141], v[148:149], v[130:131] op_sel_hi:[0,1]\n"
        "\tv_pk_fma_f32 v[148:149], v[148:149], v[130:131], v[140:141] op_sel:[1,1,0] op_sel_hi:[1,0,1] neg_lo:[1,0,0]\n"
        "\tv_cvt_pk_bf16_f32 v146, v148, v149\n"
        "\tds_write_b32 v190, v146 offset:3072\n"
        "\tv_pk_mul_f32 v[140:141], v[148:149], v[138:139] op_sel_hi:[0,1]\n"
        "\tv_pk_fma_f32 v[150:151], v[148:149], v[138:139], v[140:141] op_sel:[1,1,0] op_sel_hi:[1,0,1] neg_lo:[1,0,0]\n"
        "\tv_cvt_pk_bf16_f32 v147, v150, v151\n"
        "\tds_write_b32 v190, v147 offset:7168\n"
        "\ts_waitcnt vmcnt(8)\n"
        "\ts_barrier\n"
        "\tds_read_b64_tr_b16 v[160:161], v36 offset:0\n"
        "\tds_read_b64_tr_b16 v[162:163], v36 offset:3584\n"
        "\tds_read_b64_tr_b16 v[164:165], v36 offset:128\n"
        "\tds_read_b64_tr_b16 v[166:167], v36 offset:3712\n"
        "\tv_pk_mul_f32 v[140:141], v[148:149], v[130:131] op_sel_hi:[0,1]\n"
        "\tv_pk_fma_f32 v[148:149], v[148:149], v[130:131], v[140:141] op_sel:[1,1,0] op_sel_hi:[1,0,1] neg_lo:[1,0,0]\n"
        "\tv_cvt_pk_bf16_f32 v146, v148, v149\n"
        "\tds_write_b32 v191, v146 offset:3584\n"
        "\tv_pk_mul_f32 v[140:141], v[148:149], v[138:139] op_sel_hi:[0,1]\n"
        "\tv_pk_fma_f32 v[150:151], v[148:149], v[138:139], v[140:141] op_sel:[1,1,0] op_sel_hi:[1,0,1] neg_lo:[1,0,0]\n"
        "\tv_cvt_pk_bf16_f32 v147, v150, v151\n"
        "\tds_write_b32 v191, v147 offset:7680\n"
        "\tv_cvt_pk_bf16_f32 v146, v152, v153\n"
        "\tds_write_b32 v184, v146 offset:8192\n"
        "\ts_waitcnt lgkmcnt(3)\n"
        "\tv_mfma_f32_16x16x32_bf16 v[168:171], a[224:227], v[160:163], 0\n"
        "\tv_mfma_f32_16x16x32_bf16 v[172:175], a[224:227], v[164:167], 0\n"
        "\tbuffer_store_dwordx4 v[160:163], v1, s[8:11], s29 offen sc1\n"
        "\tv_pk_mul_f32 v[140:141], v[152:153], v[132:133] op_sel_hi:[0,1]\n"
        "\tv_pk_fma_f32 v[148:149], v[152:153], v[132:133], v[140:141] op_sel:[1,1,0] op_sel_hi:[1,0,1] neg_lo:[1,0,0]\n"
        "\tv_cvt_pk_bf16_f32 v147, v148, v149\n"
        "\tds_write_b32 v185, v147 offset:8704\n"
        "\tv_pk_mul_f32 v[140:141], v[148:149], v[132:133] op_sel_hi:[0,1]\n"
        "\tv_pk_fma_f32 v[148:149], v[148:149], v[132:133], v[140:141] op_sel:[1,1,0] op_sel_hi:[1,0,1] neg_lo:[1,0,0]\n"
        "\tv_cvt_pk_bf16_f32 v146, v148, v149\n"
        "\tds_write_b32 v186, v146 offset:9216\n"
        "\tv_pk_mul_f32 v[140:141], v[148:149], v[132:133] op_sel_hi:[0,1]\n"
        "\tv_pk_fma_f32 v[148:149], v[148:149], v[132:133], v[140:141] op_sel:[1,1,0] op_sel_hi:[1,0,1] neg_lo:[1,0,0]\n"
        "\tv_cvt_pk_bf16_f32 v147, v148, v149\n"
        "\tds_write_b32 v187, v147 offset:9728\n"
        "\tv_cvt_pk_bf16_f32 v169, v168, v168\n"
        "\tv_lshlrev_b32 v169, 16, v169\n"
        "\tv_sub_f32 v168, v168, v169\n"
        "\tv_cvt_pk_bf16_f32 v168, v169, v168\n"
        "\tv_cvt_pk_bf16_f32 v173, v172, v172\n"
        "\tv_lshlrev_b32 v173, 16, v173\n"
        "\tv_sub_f32 v172, v172, v173\n"
        "\tv_cvt_pk_bf16_f32 v172, v173, v172\n"
        "\tbuffer_store_dword v168, v37, s[8:11], s30 offen sc1\n"
        "\ts_add_u32 s30, s30, 4096\n"
        "\tbuffer_store_dword v172, v39, s[8:11], s30 offen sc1\n"
        "\tv_pk_mul_f32 v[140:141], v[152:153], v[138:139] op_sel_hi:[0,1]\n"
        "\tv_pk_fma_f32 v[152:153], v[152:153], v[138:139], v[140:141] op_sel:[1,1,0] op_sel_hi:[1,0,1] neg_lo:[1,0,0]\n"
        "\tv_cvt_pk_bf16_f32 v146, v152, v153\n"
        "\tds_write_b32 v188, v146 offset:10240\n"
        "\tv_pk_mul_f32 v[140:141], v[152:153], v[132:133] op_sel_hi:[0,1]\n"
        "\tv_pk_fma_f32 v[148:149], v[152:153], v[132:133], v[140:141] op_sel:[1,1,0] op_sel_hi:[1,0,1] neg_lo:[1,0,0]\n"
        "\tv_cvt_pk_bf16_f32 v147, v148, v149\n"
        "\tds_write_b32 v189, v147 offset:10752\n"
        "\tv_pk_mul_f32 v[140:141], v[148:149], v[132:133] op_sel_hi:[0,1]\n"
        "\tv_pk_fma_f32 v[148:149], v[148:149], v[132:133], v[140:141] op_sel:[1,1,0] op_sel_hi:[1,0,1] neg_lo:[1,0,0]\n"
        "\tv_cvt_pk_bf16_f32 v146, v148, v149\n"
        "\tds_write_b32 v190, v146 offset:11264\n"
        "\tv_pk_mul_f32 v[140:141], v[148:149], v[132:133] op_sel_hi:[0,1]\n"
        "\tv_pk_fma_f32 v[148:149], v[148:149], v[132:133], v[140:141] op_sel:[1,1,0] op_sel_hi:[1,0,1] neg_lo:[1,0,0]\n"
        "\tv_cvt_pk_bf16_f32 v147, v148, v149\n"
        "\tds_write_b32 v191, v147 offset:11776\n"
        "\tv_pk_mul_f32 v[140:141], v[152:153], v[138:139] op_sel_hi:[0,1]\n"
        "\tv_pk_fma_f32 v[152:153], v[152:153], v[138:139], v[140:141] op_sel:[1,1,0] op_sel_hi:[1,0,1] neg_lo:[1,0,0]\n"
        "\tv_cvt_pk_bf16_f32 v146, v152, v153\n"
        "\tds_write_b32 v185, v146 offset:12800\n"
        "\tv_pk_mul_f32 v[140:141], v[152:153], v[134:135] op_sel_hi:[0,1]\n"
        "\tv_pk_fma_f32 v[148:149], v[152:153], v[134:135], v[140:141] op_sel:[1,1,0] op_sel_hi:[1,0,1] neg_lo:[1,0,0]\n"
        "\tv_cvt_pk_bf16_f32 v147, v148, v149\n"
        "\tds_write_b32 v186, v147 offset:13312\n"
        "\tv_pk_mul_f32 v[140:141], v[152:153], v[138:139] op_sel_hi:[0,1]\n"
        "\tv_pk_fma_f32 v[152:153], v[152:153], v[138:139], v[140:141] op_sel:[1,1,0] op_sel_hi:[1,0,1] neg_lo:[1,0,0]\n"
        "\tv_cvt_pk_bf16_f32 v146, v152, v153\n"
        "\tds_write_b32 v187, v146 offset:13824\n"
        "\ts_add_u32 s29, s29, 4096\n"
        "\tbuffer_store_dwordx4 v[164:167], v38, s[8:11], s29 offen sc1\n"
        "\tv_pk_mul_f32 v[140:141], v[152:153], v[134:135] op_sel_hi:[0,1]\n"
        "\tv_pk_fma_f32 v[148:149], v[152:153], v[134:135], v[140:141] op_sel:[1,1,0] op_sel_hi:[1,0,1] neg_lo:[1,0,0]\n"
        "\tv_cvt_pk_bf16_f32 v147, v148, v149\n"
        "\tds_write_b32 v188, v147 offset:14336\n"
        "\tv_pk_mul_f32 v[140:141], v[152:153], v[138:139] op_sel_hi:[0,1]\n"
        "\tv_pk_fma_f32 v[152:153], v[152:153], v[138:139], v[140:141] op_sel:[1,1,0] op_sel_hi:[1,0,1] neg_lo:[1,0,0]\n"
        "\tv_cvt_pk_bf16_f32 v146, v152, v153\n"
        "\tds_write_b32 v189, v146 offset:14848\n"
        "\tv_pk_mul_f32 v[140:141], v[152:153], v[134:135] op_sel_hi:[0,1]\n"
        "\tv_pk_fma_f32 v[148:149], v[152:153], v[134:135], v[140:141] op_sel:[1,1,0] op_sel_hi:[1,0,1] neg_lo:[1,0,0]\n"
        "\tv_cvt_pk_bf16_f32 v147, v148, v149\n"
        "\tds_write_b32 v190, v147 offset:15360\n"
        "\tv_pk_mul_f32 v[140:141], v[152:153], v[138:139] op_sel_hi:[0,1]\n"
        "\tv_pk_fma_f32 v[152:153], v[152:153], v[138:139], v[140:141] op_sel:[1,1,0] op_sel_hi:[1,0,1] neg_lo:[1,0,0]\n"
        "\tv_cvt_pk_bf16_f32 v146, v152, v153\n"
        "\tds_write_b32 v191, v146 offset:15872\n"
        "\tv_pk_mul_f32 v[140:141], v[152:153], v[134:135] op_sel_hi:[0,1]\n"
        "\tv_pk_fma_f32 v[148:149], v[152:153], v[134:135], v[140:141] op_sel:[1,1,0] op_sel_hi:[1,0,1] neg_lo:[1,0,0]\n"
        "\tv_cvt_pk_bf16_f32 v147, v148, v149\n"
        "\tds_write_b32 v184, v147 offset:16384\n"
        "\tv_pk_mul_f32 v[140:141], v[152:153], v[138:139] op_sel_hi:[0,1]\n"
        "\tv_pk_fma_f32 v[152:153], v[152:153], v[138:139], v[140:141] op_sel:[1,1,0] op_sel_hi:[1,0,1] neg_lo:[1,0,0]\n"
        "\tv_cvt_pk_bf16_f32 v146, v152, v153\n"
        "\tds_write_b32 v185, v146 offset:16896\n"
        "\tv_pk_mul_f32 v[140:141], v[152:153], v[138:139] op_sel_hi:[0,1]\n"
        "\tv_pk_fma_f32 v[152:153], v[152:153], v[138:139], v[140:141] op_sel:[1,1,0] op_sel_hi:[1,0,1] neg_lo:[1,0,0]\n"
        "\tv_cvt_pk_bf16_f32 v147, v152, v153\n"
        "\tds_write_b32 v186, v147 offset:17408\n"
        "\tv_pk_mul_f32 v[140:141], v[152:153], v[138:139] op_sel_hi:[0,1]\n"
        "\tv_pk_fma_f32 v[152:153], v[152:153], v[138:139], v[140:141] op_sel:[1,1,0] op_sel_hi:[1,0,1] neg_lo:[1,0,0]\n"
        "\tv_cvt_pk_bf16_f32 v146, v152, v153\n"
        "\tds_write_b32 v187, v146 offset:17920\n"
        "\tv_pk_mul_f32 v[140:141], v[152:153], v[138:139] op_sel_hi:[0,1]\n"
        "\tv_pk_fma_f32 v[152:153], v[152:153], v[138:139], v[140:141] op_sel:[1,1,0] op_sel_hi:[1,0,1] neg_lo:[1,0,0]\n"
        "\tv_cvt_pk_bf16_f32 v147, v152, v153\n"
        "\tds_write_b32 v188, v147 offset:18432\n"
        "\tv_pk_mul_f32 v[140:141], v[152:153], v[138:139] op_sel_hi:[0,1]\n"
        "\tv_pk_fma_f32 v[152:153], v[152:153], v[138:139], v[140:141] op_sel:[1,1,0] op_sel_hi:[1,0,1] neg_lo:[1,0,0]\n"
        "\tv_cvt_pk_bf16_f32 v146, v152, v153\n"
        "\tds_write_b32 v189, v146 offset:18944\n"
        "\tv_pk_mul_f32 v[140:141], v[152:153], v[138:139] op_sel_hi:[0,1]\n"
        "\tv_pk_fma_f32 v[152:153], v[152:153], v[138:139], v[140:141] op_sel:[1,1,0] op_sel_hi:[1,0,1] neg_lo:[1,0,0]\n"
        "\tv_cvt_pk_bf16_f32 v147, v152, v153\n"
        "\tds_write_b32 v190, v147 offset:19456\n"
        "\tv_pk_mul_f32 v[140:141], v[152:153], v[138:139] op_sel_hi:[0,1]\n"
        "\tv_pk_fma_f32 v[152:153], v[152:153], v[138:139], v[140:141] op_sel:[1,1,0] op_sel_hi:[1,0,1] neg_lo:[1,0,0]\n"
        "\tv_cvt_pk_bf16_f32 v146, v152, v153\n"
        "\tds_write_b32 v191, v146 offset:19968\n"
        "\tv_pk_mul_f32 v[140:141], v[152:153], v[138:139] op_sel_hi:[0,1]\n"
        "\tv_pk_fma_f32 v[152:153], v[152:153], v[138:139], v[140:141] op_sel:[1,1,0] op_sel_hi:[1,0,1] neg_lo:[1,0,0]\n"
        "\tv_cvt_pk_bf16_f32 v147, v152, v153\n"
        "\tds_write_b32 v184, v147 offset:20480\n"
        "\tv_pk_mul_f32 v[140:141], v[152:153], v[138:139] op_sel_hi:[0,1]\n"
        "\tv_pk_fma_f32 v[152:153], v[152:153], v[138:139], v[140:141] op_sel:[1,1,0] op_sel_hi:[1,0,1] neg_lo:[1,0,0]\n"
        "\tv_cvt_pk_bf16_f32 v146, v152, v153\n"
        "\tds_write_b32 v184, v146 offset:12288\n"
        "\tv_xor_b32 v60, 64, v202\n"
        "\tv_xor_b32 v61, 128, v202\n"
        "\tv_xor_b32 v62, 192, v202\n"
        "\tv_xor_b32 v63, 64, v200\n"
        "\tv_xor_b32 v64, 128, v200\n"
        "\tv_xor_b32 v65, 192, v200\n"
        "\tv_xor_b32 v66, 64, v201\n"
        "\tv_xor_b32 v67, 128, v201\n"
        "\tv_xor_b32 v68, 192, v201\n"
        "\ts_lshr_b32 s59, s2, 6\n"
        "\ts_add_u32 s57, s59, 0\n"
        "\ts_and_b32 s57, s57, 3\n"
        "\ts_mul_i32 s24, s57, 3072\n"
        "\ts_mul_i32 s57, s57, 114688\n"
        "\ts_add_u32 s36, s57, s64\n"
        "\ts_add_u32 s57, s59, 1\n"
        "\ts_and_b32 s57, s57, 3\n"
        "\ts_mul_i32 s25, s57, 3072\n"
        "\ts_mul_i32 s57, s57, 114688\n"
        "\ts_add_u32 s37, s57, s64\n"
        "\ts_add_u32 s57, s59, 2\n"
        "\ts_and_b32 s57, s57, 3\n"
        "\ts_mul_i32 s26, s57, 3072\n"
        "\ts_mul_i32 s57, s57, 114688\n"
        "\ts_add_u32 s38, s57, s64\n"
        "\ts_add_u32 s57, s59, 3\n"
        "\ts_and_b32 s57, s57, 3\n"
        "\ts_mul_i32 s27, s57, 3072\n"
        "\ts_mul_i32 s57, s57, 114688\n"
        "\ts_add_u32 s39, s57, s64\n"
        "\ts_waitcnt lgkmcnt(0)\n"
        "\ts_barrier\n"
        "\tds_read_b128 v[108:111], v202 offset:0\n"
        "\tds_read_b128 v[112:115], v200 offset:0\n"
        "\tds_read_b128 v[116:119], v201 offset:0\n"
        "\tds_read_b128 v[120:123], v60 offset:0\n"
        "\tds_read_b128 v[124:127], v63 offset:0\n"
        "\tds_read_b128 v[128:131], v66 offset:0\n"
        "\tds_read_b128 v[132:135], v61 offset:0\n"
        "\tds_read_b128 v[136:139], v64 offset:0\n"
        "\tds_read_b128 v[140:143], v67 offset:0\n"
        "\tds_read_b128 v[144:147], v62 offset:0\n"
        "\tds_read_b128 v[148:151], v65 offset:0\n"
        "\tds_read_b128 v[152:155], v68 offset:0\n"
        "\ts_add_u32 m0, s66, 0\n"
        "\ts_add_u32 s96, s36, 0\n"
        "\tbuffer_load_dwordx4 v244, s[12:15], s96 offen offset:0 lds\n"
        "\tv_lshrrev_b32 v252, 1, v0\n"
        "\tv_mul_u32_u24 v252, 4160, v252\n"
        "\tv_and_b32 v253, 16, v24\n"
        "\tv_add_u32 v252, v253, v252\n"
        "\tv_add_u32 v252, 118784, v252\n"
        "\tv_add_u32 v253, 146944, v24\n"
        "\tv_cmp_lt_u32 vcc, 93, v0\n"
        "\tv_add_u32 v254, 4096, v253\n"
        "\tv_subrev_u32 v203, 64, v0\n"
        "\tv_cndmask_b32 v253, v253, v254, vcc\n"
        "\tv_cmp_gt_u32 vcc, 60, v203\n"
        "\tv_and_b32 v254, 0x1f0, v1\n"
        "\tv_add_u32 v254, 163328, v254\n"
        "\tv_cndmask_b32 v254, v254, v253, vcc\n"
        "\tv_cmp_gt_u32 vcc, 16, v0\n"
        "\ts_nop 1\n"
        "\tv_cndmask_b32 v240, v254, v252, vcc\n"
        "\ts_and_b32 s57, s3, 1\n"
        "\ts_lshl_b32 s57, s57, 8\n"
        "\ts_mul_i32 s58, s62, 16640\n"
        "\ts_add_u32 s58, s58, 114688\n"
        "\ts_waitcnt lgkmcnt(10)\n"
        "\tv_mfma_f32_16x16x32_bf16 v[100:103], v[112:115], v[108:111], 0\n"
        "\tds_read_b128 v[44:47], v202 offset:256\n"
        "\tds_read_b128 v[48:51], v200 offset:256\n"
        "\ts_waitcnt lgkmcnt(11)\n"
        "\tv_mfma_f32_16x16x32_bf16 v[104:107], v[116:119], v[108:111], 0\n"
        "\tds_read_b128 v[52:55], v201 offset:256\n"
        "\ts_waitcnt lgkmcnt(10)\n"
        "\tv_mfma_f32_16x16x32_bf16 v[100:103], v[124:127], v[120:123], v[100:103]\n"
        "\tds_read_b128 v[70:73], v60 offset:256\n"
        "\tds_read_b128 v[74:77], v63 offset:256\n"
        "\ts_waitcnt lgkmcnt(11)\n"
        "\tv_mfma_f32_16x16x32_bf16 v[104:107], v[128:131], v[120:123], v[104:107]\n"
        "\tds_read_b128 v[78:81], v66 offset:256\n"
        "\ts_waitcnt lgkmcnt(10)\n"
        "\tv_mfma_f32_16x16x32_bf16 v[100:103], v[136:139], v[132:135], v[100:103]\n"
        "\tds_read_b128 v[82:85], v61 offset:256\n"
        "\tds_read_b128 v[86:89], v64 offset:256\n"
        "\ts_waitcnt lgkmcnt(11)\n"
        "\tv_mfma_f32_16x16x32_bf16 v[104:107], v[140:143], v[132:135], v[104:107]\n"
        "\tds_read_b128 v[90:93], v67 offset:256\n"
        "\ts_waitcnt lgkmcnt(10)\n"
        "\tv_mfma_f32_16x16x32_bf16 v[100:103], v[148:151], v[144:147], v[100:103]\n"
        "\tds_read_b128 v[56:59], v62 offset:256\n"
        "\tds_read_b128 v[94:97], v65 offset:256\n"
        "\ts_waitcnt lgkmcnt(11)\n"
        "\tv_mfma_f32_16x16x32_bf16 v[104:107], v[152:155], v[144:147], v[104:107]\n"
        "\tds_read_b128 v[212:215], v68 offset:256\n"
        "\ts_waitcnt lgkmcnt(10)\n"
        "\tv_mfma_f32_16x16x32_bf16 v[100:103], v[48:51], v[44:47], v[100:103]\n"
        "\ts_waitcnt lgkmcnt(9)\n"
        "\tv_mfma_f32_16x16x32_bf16 v[104:107], v[52:55], v[44:47], v[104:107]\n"
        "\tbuffer_load_dwordx4 v245, s[12:15], s96 offen offset:1024 lds\n"
        "\ts_waitcnt lgkmcnt(7)\n"
        "\tv_mfma_f32_16x16x32_bf16 v[100:103], v[74:77], v[70:73], v[100:103]\n"
        "\ts_waitcnt lgkmcnt(6)\n"
        "\tv_mfma_f32_16x16x32_bf16 v[104:107], v[78:81], v[70:73], v[104:107]\n"
        "\ts_waitcnt lgkmcnt(4)\n"
        "\tv_mfma_f32_16x16x32_bf16 v[100:103], v[86:89], v[82:85], v[100:103]\n"
        "\ts_waitcnt lgkmcnt(3)\n"
        "\tv_mfma_f32_16x16x32_bf16 v[104:107], v[90:93], v[82:85], v[104:107]\n"
        "\ts_waitcnt lgkmcnt(1)\n"
        "\tv_mfma_f32_16x16x32_bf16 v[100:103], v[94:97], v[56:59], v[100:103]\n"
        "\ts_waitcnt lgkmcnt(0)\n"
        "\tv_mfma_f32_16x16x32_bf16 v[104:107], v[212:215], v[56:59], v[104:107]\n"
        "\ts_nop 5\n"
        "\tv_cvt_pk_bf16_f32 v100, v100, v101\n"
        "\tv_cvt_pk_bf16_f32 v101, v102, v103\n"
        "\tv_cvt_pk_bf16_f32 v104, v104, v105\n"
        "\tv_cvt_pk_bf16_f32 v105, v106, v107\n"
        "\tds_write_b16 v204, v100\n"
        "\tds_write_b16_d16_hi v205, v100\n"
        "\tds_write_b16 v206, v101\n"
        "\tds_write_b16_d16_hi v207, v101\n"
        "\tds_write_b16 v208, v104\n"
        "\tds_write_b16_d16_hi v209, v104\n"
        "\tds_write_b16 v210, v105\n"
        "\tds_write_b16_d16_hi v211, v105\n"
        "\tbuffer_store_dwordx2 v[20:21], v19, s[8:11], s75 offen sc1\n"
        "\ts_waitcnt vmcnt(3) lgkmcnt(0)\n"
        "\ts_barrier\n"
        "\tds_read_b32 v100, v229\n"
        "\tds_read_b32 v101, v230\n"
        "\tds_read_b32 v102, v231\n"
        "\tds_read_b32 v103, v232\n"
        "\tds_read_b32 v104, v233\n"
        "\tds_read_b32 v105, v234\n"
        "\tds_read_b32 v106, v235\n"
        "\tds_read_b32 v107, v236\n"
        "\tds_read_b32 v108, v237\n"
        "\tds_read_b32 v109, v238\n"
        "\tds_read_b32 v110, v239\n"
        "\tbuffer_load_dwordx4 v[156:159], v24, s[8:11], s97 offen sc1\n"
        "\tbuffer_load_dwordx4 v[160:163], v24, s[8:11], s98 offen sc1\n"
        "\tbuffer_load_dwordx4 v[164:167], v24, s[8:11], s99 offen sc1\n"
        "\tbuffer_load_dwordx4 v[168:171], v24, s[8:11], s63 offen sc1\n"
        "\tbuffer_load_dwordx4 v[172:175], v24, s[8:11], s68 offen sc1\n"
        "\tbuffer_load_dwordx4 v[176:179], v24, s[8:11], s65 offen sc1\n"
        "\tbuffer_load_dwordx2 v[180:181], v228, s[8:11], s69 offen sc1\n"
        "\ts_sub_u32 s58, s58, s57\n"
        "\tv_and_b32 v252, 3, v0\n"
        "\tv_mul_u32_u24 v252, 4160, v252\n"
        "\tv_lshl_add_u32 v252, v28, 4, v252\n"
        "\tv_and_b32 v253, 12, v0\n"
        "\tv_lshlrev_b32 v253, 1, v253\n"
        "\tv_sub_u32 v252, v252, v253\n"
        "\tv_add_u32 v41, s58, v252\n"
        "\ts_mul_i32 s58, s62, 4576\n"
        "\ts_add_u32 s58, s58, 147968\n"
        "\ts_sub_u32 s58, s58, s57\n"
        "\tv_lshlrev_b32 v252, 5, v27\n"
        "\tv_lshl_add_u32 v253, v28, 4, v252\n"
        "\tv_add_u32 v42, s58, v253\n"
        "\ts_lshl_b32 s58, s3, 12\n"
        "\ts_add_u32 s58, s58, 114688\n"
        "\tv_lshl_add_u32 v253, v28, 3, v252\n"
        "\tv_and_b32 v252, 4, v0\n"
        "\tv_lshlrev_b32 v252, 2, v252\n"
        "\tv_xor_b32 v253, v253, v252\n"
        "\tv_add_u32 v43, s58, v253\n"
        "\tv_and_b32 v252, 8, v0\n"
        "\tv_lshlrev_b32 v252, 1, v252\n"
        "\tv_xor_b32 v252, v252, v24\n"
        "\tv_add_u32 v31, 114688, v252\n"
        "\tv_mov_b32 v128, 0\n"
        "\tv_mov_b32 v129, 0\n"
        "\tv_mov_b32 v130, 0\n"
        "\tv_mov_b32 v131, 0\n"
        "\tv_add_u32 v127, 114688, v24\n"
        "\tv_add_u32 v126, 148448, v24\n"
        "\tds_write_b128 v240, v[128:131]\n"
        "\ts_waitcnt lgkmcnt(4)\n"
        "\tv_perm_b32 v112, v106, v107, s84\n"
        "\tv_perm_b32 v113, v104, v105, s84\n"
        "\tv_perm_b32 v114, v102, v103, s84\n"
        "\tv_perm_b32 v115, v100, v101, s84\n"
        "\tds_write_b128 v127, v[112:115] offset:0\n"
        "\tbuffer_load_dwordx4 v246, s[12:15], s96 offen offset:2048 lds\n"
        "\tv_perm_b32 v116, v106, v107, s85\n"
        "\tv_perm_b32 v117, v104, v105, s85\n"
        "\tv_perm_b32 v118, v102, v103, s85\n"
        "\tv_perm_b32 v119, v100, v101, s85\n"
        "\tds_write_b128 v127, v[116:119] offset:16640\n"
        "\tbuffer_load_dwordx4 v247, s[12:15], s96 offen offset:3072 lds\n"
        "\ts_waitcnt lgkmcnt(5)\n"
        "\tv_perm_b32 v120, v107, v108, s84\n"
        "\tv_perm_b32 v121, v105, v106, s84\n"
        "\tv_perm_b32 v122, v103, v104, s84\n"
        "\tv_perm_b32 v123, v101, v102, s84\n"
        "\tds_write_b128 v127, v[120:123] offset:4160\n"
        "\ts_add_u32 m0, s66, 4096\n"
        "\ts_add_u32 s96, s36, 32768\n"
        "\tbuffer_load_dwordx4 v244, s[12:15], s96 offen offset:0 lds\n"
        "\tv_perm_b32 v112, v107, v108, s85\n"
        "\tv_perm_b32 v113, v105, v106, s85\n"
        "\tv_perm_b32 v114, v103, v104, s85\n"
        "\tv_perm_b32 v115, v101, v102, s85\n"
        "\tds_write_b128 v127, v[112:115] offset:20800\n"
        "\tbuffer_load_dwordx4 v245, s[12:15], s96 offen offset:1024 lds\n"
        "\ts_waitcnt lgkmcnt(6)\n"
        "\tv_perm_b32 v116, v108, v109, s84\n"
        "\tv_perm_b32 v117, v106, v107, s84\n"
        "\tv_perm_b32 v118, v104, v105, s84\n"
        "\tv_perm_b32 v119, v102, v103, s84\n"
        "\tds_write_b128 v127, v[116:119] offset:8320\n"
        "\tbuffer_load_dwordx4 v246, s[12:15], s96 offen offset:2048 lds\n"
        "\tv_perm_b32 v120, v108, v109, s85\n"
        "\tv_perm_b32 v121, v106, v107, s85\n"
        "\tv_perm_b32 v122, v104, v105, s85\n"
        "\tv_perm_b32 v123, v102, v103, s85\n"
        "\tds_write_b128 v127, v[120:123] offset:24960\n"
        "\tbuffer_load_dwordx4 v247, s[12:15], s96 offen offset:3072 lds\n"
        "\ts_waitcnt vmcnt(6)\n"
        "\tv_mad_u64_u32 v[182:183], s[94:95], v172, v220, 0\n"
        "\tv_mad_u64_u32 v[182:183], s[94:95], v173, v221, v[182:183]\n"
        "\tv_mad_u64_u32 v[182:183], s[94:95], v174, v222, v[182:183]\n"
        "\tv_mad_u64_u32 v[182:183], s[94:95], v175, v223, v[182:183]\n"
        "\tv_mad_u64_u32 v[182:183], s[94:95], v176, v224, v[182:183]\n"
        "\tv_mad_u64_u32 v[182:183], s[94:95], v177, v225, v[182:183]\n"
        "\tv_mad_u64_u32 v[182:183], s[94:95], v178, v226, v[182:183]\n"
        "\tv_mad_u64_u32 v[182:183], s[94:95], v179, v227, v[182:183]\n"
        "\ts_nop 1\n"
        "\tv_mov_b32_dpp v184, v182 quad_perm:[1,0,3,2] row_mask:0xf bank_mask:0xf\n"
        "\tv_mov_b32_dpp v185, v183 quad_perm:[1,0,3,2] row_mask:0xf bank_mask:0xf\n"
        "\ts_add_u32 m0, s66, 8192\n"
        "\ts_add_u32 s96, s37, 0\n"
        "\tbuffer_load_dwordx4 v244, s[12:15], s96 offen offset:0 lds\n"
        "\tv_lshl_add_u64 v[182:183], v[182:183], 0, v[184:185]\n"
        "\ts_nop 1\n"
        "\tv_mov_b32_dpp v184, v182 quad_perm:[2,3,0,1] row_mask:0xf bank_mask:0xf\n"
        "\tv_mov_b32_dpp v185, v183 quad_perm:[2,3,0,1] row_mask:0xf bank_mask:0xf\n"
        "\tv_lshl_add_u64 v[182:183], v[182:183], 0, v[184:185]\n"
        "\tv_lshl_add_u64 v[182:183], v[182:183], 0, v[20:21]\n"
        "\tv_cmp_ne_u64 vcc, v[182:183], v[180:181]\n"
        "\tv_mad_u64_u32 v[184:185], s[94:95], v156, s86, v[20:21]\n"
        "\tv_mad_u64_u32 v[184:185], s[94:95], v157, s87, v[184:185]\n"
        "\tv_cmp_ne_u64 s[58:59], v[184:185], v[158:159]\n"
        "\ts_or_b64 vcc, vcc, s[58:59]\n"
        "\tbuffer_load_dwordx4 v245, s[12:15], s96 offen offset:1024 lds\n"
        "\tv_mad_u64_u32 v[184:185], s[94:95], v160, s86, v[20:21]\n"
        "\tv_mad_u64_u32 v[184:185], s[94:95], v161, s87, v[184:185]\n"
        "\tv_cmp_ne_u64 s[58:59], v[184:185], v[162:163]\n"
        "\ts_or_b64 vcc, vcc, s[58:59]\n"
        "\tv_mad_u64_u32 v[184:185], s[94:95], v164, s86, v[20:21]\n"
        "\tv_mad_u64_u32 v[184:185], s[94:95], v165, s87, v[184:185]\n"
        "\tv_cmp_ne_u64 s[58:59], v[184:185], v[166:167]\n"
        "\ts_or_b64 vcc, vcc, s[58:59]\n"
        "\tv_mad_u64_u32 v[184:185], s[94:95], v168, s86, v[20:21]\n"
        "\tv_mad_u64_u32 v[184:185], s[94:95], v169, s87, v[184:185]\n"
        "\tbuffer_load_dwordx4 v246, s[12:15], s96 offen offset:2048 lds\n"
        "\tv_cmp_ne_u64 s[58:59], v[184:185], v[170:171]\n"
        "\ts_or_b64 vcc, vcc, s[58:59]\n"
        "\ts_cbranch_vccnz LRx\n"
        "LBx:\n"
        "\ts_waitcnt lgkmcnt(7)\n"
        "\tv_perm_b32 v112, v109, v110, s84\n"
        "\tv_perm_b32 v113, v107, v108, s84\n"
        "\tv_perm_b32 v114, v105, v106, s84\n"
        "\tv_perm_b32 v115, v103, v104, s84\n"
        "\tds_write_b128 v127, v[112:115] offset:12480\n"
        "\ts_lshl_b32 s56, s82, 16\n"
        "\ts_and_b32 s58, s82, s83\n"
        "\tv_and_b32 v212, s83, v156\n"
        "\tv_and_b32 v213, s83, v157\n"
        "\tv_rcp_f32 v212, v212\n"
        "\tv_rcp_f32 v213, v213\n"
        "\tbuffer_load_dwordx4 v247, s[12:15], s96 offen offset:3072 lds\n"
        "\tv_lshlrev_b32 v210, 16, v156\n"
        "\tv_lshlrev_b32 v211, 16, v157\n"
        "\tv_pk_mul_f32 v[210:211], v[210:211], v[212:213] neg_lo:[1,0] neg_hi:[1,0]\n"
        "\tv_lshlrev_b32 v44, 16, v172\n"
        "\tv_and_b32 v45, s83, v172\n"
        "\tv_pk_fma_f32 v[214:215], v[44:45], v[212:213], v[210:211]\n"
        "\tv_cvt_pk_bf16_f32 v186, v214, v215\n"
        "\tv_pk_fma_f32 v[44:45], v[214:215], s[56:57], v[44:45] op_sel_hi:[1,0,1]\n"
        "\tv_lshlrev_b32 v52, 16, v176\n"
        "\tv_and_b32 v53, s83, v176\n"
        "\ts_add_u32 m0, s66, 12288\n"
        "\ts_add_u32 s96, s37, 32768\n"
        "\tbuffer_load_dwordx4 v244, s[12:15], s96 offen offset:0 lds\n"
        "\tv_pk_fma_f32 v[214:215], v[52:53], v[212:213], v[210:211]\n"
        "\tv_cvt_pk_bf16_f32 v190, v214, v215\n"
        "\tv_pk_fma_f32 v[52:53], v[214:215], s[58:59], v[52:53] op_sel_hi:[1,0,1]\n"
        "\tv_and_b32 v212, s83, v160\n"
        "\tv_and_b32 v213, s83, v161\n"
        "\tv_rcp_f32 v212, v212\n"
        "\tv_rcp_f32 v213, v213\n"
        "\tv_lshlrev_b32 v210, 16, v160\n"
        "\tv_lshlrev_b32 v211, 16, v161\n"
        "\tv_pk_mul_f32 v[210:211], v[210:211], v[212:213] neg_lo:[1,0] neg_hi:[1,0]\n"
        "\tbuffer_load_dwordx4 v245, s[12:15], s96 offen offset:1024 lds\n"
        "\tv_lshlrev_b32 v46, 16, v173\n"
        "\tv_and_b32 v47, s83, v173\n"
        "\tv_pk_fma_f32 v[214:215], v[46:47], v[212:213], v[210:211]\n"
        "\tv_cvt_pk_bf16_f32 v187, v214, v215\n"
        "\tv_pk_fma_f32 v[46:47], v[214:215], s[56:57], v[46:47] op_sel_hi:[1,0,1]\n"
        "\tv_lshlrev_b32 v54, 16, v177\n"
        "\tv_and_b32 v55, s83, v177\n"
        "\tv_pk_fma_f32 v[214:215], v[54:55], v[212:213], v[210:211]\n"
        "\tv_perm_b32 v116, v109, v110, s85\n"
        "\tv_perm_b32 v117, v107, v108, s85\n"
        "\tv_perm_b32 v118, v105, v106, s85\n"
        "\tv_perm_b32 v119, v103, v104, s85\n"
        "\tds_write_b128 v127, v[116:119] offset:29120\n"
        "\tv_cvt_pk_bf16_f32 v191, v214, v215\n"
        "\tv_pk_fma_f32 v[54:55], v[214:215], s[58:59], v[54:55] op_sel_hi:[1,0,1]\n"
        "\tbuffer_load_dwordx4 v246, s[12:15], s96 offen offset:2048 lds\n"
        "\tv_and_b32 v212, s83, v164\n"
        "\tv_and_b32 v213, s83, v165\n"
        "\tv_rcp_f32 v212, v212\n"
        "\tv_rcp_f32 v213, v213\n"
        "\tv_lshlrev_b32 v210, 16, v164\n"
        "\tv_lshlrev_b32 v211, 16, v165\n"
        "\tv_pk_mul_f32 v[210:211], v[210:211], v[212:213] neg_lo:[1,0] neg_hi:[1,0]\n"
        "\tv_lshlrev_b32 v48, 16, v174\n"
        "\tv_and_b32 v49, s83, v174\n"
        "\tv_pk_fma_f32 v[214:215], v[48:49], v[212:213], v[210:211]\n"
        "\tbuffer_load_dwordx4 v247, s[12:15], s96 offen offset:3072 lds\n"
        "\tv_cvt_pk_bf16_f32 v188, v214, v215\n"
        "\tv_pk_fma_f32 v[48:49], v[214:215], s[56:57], v[48:49] op_sel_hi:[1,0,1]\n"
        "\tv_lshlrev_b32 v56, 16, v178\n"
        "\tv_and_b32 v57, s83, v178\n"
        "\tv_pk_fma_f32 v[214:215], v[56:57], v[212:213], v[210:211]\n"
        "\tv_cvt_pk_bf16_f32 v192, v214, v215\n"
        "\tv_pk_fma_f32 v[56:57], v[214:215], s[58:59], v[56:57] op_sel_hi:[1,0,1]\n"
        "\tv_and_b32 v212, s83, v168\n"
        "\tv_and_b32 v213, s83, v169\n"
        "\tv_rcp_f32 v212, v212\n"
        "\ts_add_u32 m0, s66, 16384\n"
        "\ts_add_u32 s96, s38, 0\n"
        "\tbuffer_load_dwordx4 v244, s[12:15], s96 offen offset:0 lds\n"
        "\tv_rcp_f32 v213, v213\n"
        "\tv_lshlrev_b32 v210, 16, v168\n"
        "\tv_lshlrev_b32 v211, 16, v169\n"
        "\tv_pk_mul_f32 v[210:211], v[210:211], v[212:213] neg_lo:[1,0] neg_hi:[1,0]\n"
        "\tv_lshlrev_b32 v50, 16, v175\n"
        "\tv_and_b32 v51, s83, v175\n"
        "\tv_pk_fma_f32 v[214:215], v[50:51], v[212:213], v[210:211]\n"
        "\tv_cvt_pk_bf16_f32 v189, v214, v215\n"
        "\tv_pk_fma_f32 v[50:51], v[214:215], s[56:57], v[50:51] op_sel_hi:[1,0,1]\n"
        "\tv_lshlrev_b32 v58, 16, v179\n"
        "\tbuffer_load_dwordx4 v245, s[12:15], s96 offen offset:1024 lds\n"
        "\tv_and_b32 v59, s83, v179\n"
        "\tv_pk_fma_f32 v[214:215], v[58:59], v[212:213], v[210:211]\n"
        "\tv_cvt_pk_bf16_f32 v193, v214, v215\n"
        "\tv_pk_fma_f32 v[58:59], v[214:215], s[58:59], v[58:59] op_sel_hi:[1,0,1]\n"
        "\ts_memrealtime s[32:33]\n"
        "\tds_write_b128 v126, v[186:189]\n"
        "\tds_write_b128 v126, v[190:193] offset:4576\n"
        "\ts_waitcnt lgkmcnt(0)\n"
        "\ts_barrier\n"
        "\tds_read_b128 v[124:127], v42 offset:448\n"
        "\tds_read_b64 v[92:93], v41 offset:4056\n"
        "\tds_read_b64 v[94:95], v41 offset:4064\n"
        "\tds_read_b64 v[96:97], v41 offset:3544\n"
        "\tds_read_b64 v[98:99], v41 offset:3552\n"
        "\tds_read_b64 v[100:101], v41 offset:3032\n"
        "\tds_read_b64 v[102:103], v41 offset:3040\n"
        "\tds_read_b64 v[104:105], v41 offset:2520\n"
        "\tds_read_b64 v[106:107], v41 offset:2528\n"
        "\tds_read_b64 v[108:109], v41 offset:2008\n"
        "\tds_read_b64 v[110:111], v41 offset:2016\n"
        "\tds_read_b64 v[112:113], v41 offset:1496\n"
        "\tds_read_b64 v[114:115], v41 offset:1504\n"
        "\tds_read_b64 v[116:117], v41 offset:984\n"
        "\tds_read_b64 v[118:119], v41 offset:992\n"
        "\tds_read_b64 v[120:121], v41 offset:472\n"
        "\tds_read_b64 v[122:123], v41 offset:480\n"
        "\tds_read_b128 v[128:131], v42 offset:960\n"
        "\tds_read_b128 v[132:135], v42 offset:1472\n"
        "\tds_read_b128 v[136:139], v42 offset:1984\n"
        "\tds_read_b128 v[140:143], v42 offset:2496\n"
        "\tds_read_b128 v[144:147], v42 offset:3008\n"
        "\tds_read_b128 v[148:151], v42 offset:3520\n"
        "\tds_read_b128 v[152:155], v42 offset:4032\n"
        "\tbuffer_store_dwordx2 v[20:21], v18, s[8:11], 0 offen sc1\n"
        "\tv_lshrrev_b32 v230, 1, v24\n"
        "\ts_movk_i32 s60, 28672\n"
        "\ts_waitcnt lgkmcnt(15)\n"
        "\tv_mfma_f32_16x16x32_bf16 v[60:63], v[92:95], v[124:127], 0\n"
        "\tds_read_b128 v[188:191], v42 offset:384\n"
        "\ts_mov_b32 s34, 0xffff\n"
        "\tv_mfma_f32_16x16x32_bf16 v[64:67], v[96:99], v[124:127], 0\n"
        "\tds_read_b64 v[156:157], v41 offset:3992\n"
        "\ts_waitcnt vmcnt(15)\n"
        "\tds_read_b64_tr_b16 a[0:1], v248 offset:0\n"
        "\tv_mov_b32 v223, v21\n"
        "\tv_mfma_f32_16x16x32_bf16 v[68:71], v[100:103], v[124:127], 0\n"
        "\tds_read_b64_tr_b16 a[2:3], v248 offset:2048\n"
        "\tds_read_b64_tr_b16 a[4:5], v249 offset:0\n"
        "\tv_mov_b32 v222, s32\n"
        "\tv_mfma_f32_16x16x32_bf16 v[72:75], v[104:107], v[124:127], 0\n"
        "\tds_read_b64 v[158:159], v41 offset:4000\n"
        "\tds_read_b64_tr_b16 a[6:7], v249 offset:2048\n"
        "\tv_bfi_b32 v222, s34, v222, v20\n"
        "\ts_waitcnt lgkmcnt(15)\n"
        "\tv_mfma_f32_16x16x32_bf16 v[76:79], v[108:111], v[124:127], 0\n"
        "\tds_read_b64 v[160:161], v41 offset:3480\n"
        "\tds_read_b64_tr_b16 a[8:9], v250 offset:0\n"
        "\tbuffer_store_dwordx2 v[222:223], v19, s[8:11], s61 offen sc1\n"
        "\tv_mfma_f32_16x16x32_bf16 v[80:83], v[112:115], v[124:127], 0\n"
        "\tds_read_b64_tr_b16 a[10:11], v250 offset:2048\n"
        "\tds_read_b64_tr_b16 a[12:13], v251 offset:0\n"
        "\tbuffer_load_dwordx4 v246, s[12:15], s96 offen offset:2048 lds\n"
        "\tv_mfma_f32_16x16x32_bf16 v[84:87], v[116:119], v[124:127], 0\n"
        "\tds_read_b64 v[162:163], v41 offset:3488\n"
        "\tds_read_b64_tr_b16 a[14:15], v251 offset:2048\n"
        "\ts_mul_i32 s57, s3, 12288\n"
        "\ts_waitcnt lgkmcnt(15)\n"
        "\tv_mfma_f32_16x16x32_bf16 v[88:91], v[120:123], v[124:127], 0\n"
        "\tds_read_b64 v[164:165], v41 offset:2968\n"
        "\ts_waitcnt vmcnt(13)\n"
        "\tds_read_b64_tr_b16 a[16:17], v248 offset:4096\n"
        "\ts_add_u32 s67, s57, 114688\n"
        "\tv_mfma_f32_16x16x32_bf16 v[64:67], v[92:95], v[128:131], v[64:67]\n"
        "\tds_read_b64_tr_b16 a[18:19], v248 offset:6144\n"
        "\tds_read_b64_tr_b16 a[20:21], v249 offset:4096\n"
        "\tv_and_b32 v224, 1, v28\n"
        "\tv_mfma_f32_16x16x32_bf16 v[68:71], v[96:99], v[128:131], v[68:71]\n"
        "\tds_read_b64 v[166:167], v41 offset:2976\n"
        "\tds_read_b64_tr_b16 a[22:23], v249 offset:6144\n"
        "\tbuffer_load_dwordx4 v247, s[12:15], s96 offen offset:3072 lds\n"
        "\tv_mfma_f32_16x16x32_bf16 v[72:75], v[100:103], v[128:131], v[72:75]\n"
        "\tds_read_b64 v[168:169], v41 offset:2456\n"
        "\tds_read_b64_tr_b16 a[24:25], v250 offset:4096\n"
        "\ts_add_u32 m0, s66, 20480\n"
        "\tv_mfma_f32_16x16x32_bf16 v[76:79], v[104:107], v[128:131], v[76:79]\n"
        "\tds_read_b64_tr_b16 a[26:27], v250 offset:6144\n"
        "\tds_read_b64_tr_b16 a[28:29], v251 offset:4096\n"
        "\ts_add_u32 s96, s38, 32768\n"
        "\tv_mfma_f32_16x16x32_bf16 v[80:83], v[108:111], v[128:131], v[80:83]\n"
        "\tds_read_b64 v[170:171], v41 offset:2464\n"
        "\tds_read_b64_tr_b16 a[30:31], v251 offset:6144\n"
        "\tv_and_b32 v225, 3, v26\n"
        "\tv_mfma_f32_16x16x32_bf16 v[84:87], v[112:115], v[128:131], v[84:87]\n"
        "\tds_read_b64 v[172:173], v41 offset:1944\n"
        "\ts_waitcnt vmcnt(10)\n"
        "\tds_read_b64_tr_b16 a[32:33], v248 offset:8192\n"
        "\tbuffer_load_dwordx4 v244, s[12:15], s96 offen offset:0 lds\n"
        "\tv_mfma_f32_16x16x32_bf16 v[88:91], v[116:119], v[128:131], v[88:91]\n"
        "\tds_read_b64_tr_b16 a[34:35], v248 offset:10240\n"
        "\tds_read_b64_tr_b16 a[36:37], v249 offset:8192\n"
        "\tv_lshlrev_b32 v226, 1, v224\n"
        "\tv_mfma_f32_16x16x32_bf16 v[68:71], v[92:95], v[132:135], v[68:71]\n"
        "\tds_read_b64 v[174:175], v41 offset:1952\n"
        "\tds_read_b64_tr_b16 a[38:39], v249 offset:10240\n"
        "\tv_xor_b32 v225, v225, v226\n"
        "\tv_mfma_f32_16x16x32_bf16 v[72:75], v[96:99], v[132:135], v[72:75]\n"
        "\tds_read_b64 v[176:177], v41 offset:1432\n"
        "\tds_read_b64_tr_b16 a[40:41], v250 offset:8192\n"
        "\tbuffer_load_dwordx2 v[228:229], v230, s[8:11], s60 offen sc1\n"
        "\tv_mfma_f32_16x16x32_bf16 v[76:79], v[100:103], v[132:135], v[76:79]\n"
        "\tds_read_b64_tr_b16 a[42:43], v250 offset:10240\n"
        "\tds_read_b64_tr_b16 a[44:45], v251 offset:8192\n"
        "\tv_lshlrev_b32 v225, 4, v225\n"
        "\tv_mfma_f32_16x16x32_bf16 v[80:83], v[104:107], v[132:135], v[80:83]\n"
        "\tds_read_b64 v[178:179], v41 offset:1440\n"
        "\tds_read_b64_tr_b16 a[46:47], v251 offset:10240\n"
        "\tv_lshl_add_u32 v2, v29, 6, v225\n"
        "\tv_mfma_f32_16x16x32_bf16 v[84:87], v[108:111], v[132:135], v[84:87]\n"
        "\tds_read_b64 v[180:181], v41 offset:920\n"
        "\ts_waitcnt vmcnt(8)\n"
        "\tds_read_b64_tr_b16 a[48:49], v248 offset:12288\n"
        "\tv_lshl_add_u32 v225, v29, 6, v30\n"
        "\tv_mfma_f32_16x16x32_bf16 v[88:91], v[112:115], v[132:135], v[88:91]\n"
        "\tds_read_b64_tr_b16 a[50:51], v248 offset:14336\n"
        "\tds_read_b64_tr_b16 a[52:53], v249 offset:12288\n"
        "\tbuffer_load_dwordx4 v245, s[12:15], s96 offen offset:1024 lds\n"
        "\tv_mfma_f32_16x16x32_bf16 v[72:75], v[92:95], v[136:139], v[72:75]\n"
        "\tds_read_b64 v[182:183], v41 offset:928\n"
        "\tds_read_b64_tr_b16 a[54:55], v249 offset:14336\n"
        "\tv_lshl_add_u32 v225, v224, 5, v225\n"
        "\tv_mfma_f32_16x16x32_bf16 v[76:79], v[96:99], v[136:139], v[76:79]\n"
        "\tds_read_b64 v[184:185], v41 offset:408\n"
        "\tds_read_b64_tr_b16 a[56:57], v250 offset:12288\n"
        "\tv_add_u32 v3, s67, v225\n"
        "\tv_mfma_f32_16x16x32_bf16 v[80:83], v[100:103], v[136:139], v[80:83]\n"
        "\tds_read_b64_tr_b16 a[58:59], v250 offset:14336\n"
        "\tds_read_b64_tr_b16 a[60:61], v251 offset:12288\n"
        "\tv_xor_b32 v4, 32, v3\n"
        "\tv_mfma_f32_16x16x32_bf16 v[84:87], v[104:107], v[136:139], v[84:87]\n"
        "\tds_read_b64 v[186:187], v41 offset:416\n"
        "\tds_read_b64_tr_b16 a[62:63], v251 offset:14336\n"
        "\tbuffer_load_dwordx4 v246, s[12:15], s96 offen offset:2048 lds\n"
        "\tv_mfma_f32_16x16x32_bf16 v[88:91], v[108:111], v[136:139], v[88:91]\n"
        "\tds_read_b128 v[192:195], v42 offset:896\n"
        "\tv_add_u32 v6, 57344, v1\n"
        "\tv_mfma_f32_16x16x32_bf16 v[76:79], v[92:95], v[140:143], v[76:79]\n"
        "\tds_read_b128 v[196:199], v42 offset:1408\n"
        "\ts_lshr_b32 s57, s28, 7\n"
        "\tv_mfma_f32_16x16x32_bf16 v[80:83], v[96:99], v[140:143], v[80:83]\n"
        "\tds_read_b128 v[200:203], v42 offset:1920\n"
        "\tv_xor_b32 v224, s57, v26\n"
        "\tv_mfma_f32_16x16x32_bf16 v[84:87], v[100:103], v[140:143], v[84:87]\n"
        "\tds_read_b128 v[204:207], v42 offset:2432\n"
        "\tbuffer_load_dwordx4 v247, s[12:15], s96 offen offset:3072 lds\n"
        "\tv_mfma_f32_16x16x32_bf16 v[88:91], v[104:107], v[140:143], v[88:91]\n"
        "\tds_read_b128 v[208:211], v42 offset:2944\n"
        "\ts_add_u32 m0, s66, 24576\n"
        "\tv_mfma_f32_16x16x32_bf16 v[80:83], v[92:95], v[144:147], v[80:83]\n"
        "\tds_read_b128 v[212:215], v42 offset:3456\n"
        "\ts_add_u32 s96, s39, 0\n"
        "\tv_mfma_f32_16x16x32_bf16 v[84:87], v[96:99], v[144:147], v[84:87]\n"
        "\tds_read_b128 v[216:219], v42 offset:3968\n"
        "\tv_and_b32 v224, 1, v224\n"
        "\tv_mfma_f32_16x16x32_bf16 v[88:91], v[100:103], v[144:147], v[88:91]\n"
        "\tbuffer_load_dwordx4 v244, s[12:15], s96 offen offset:0 lds\n"
        "\ts_waitcnt lgkmcnt(15)\n"
        "\tv_mfma_f32_16x16x32_bf16 v[84:87], v[92:95], v[148:151], v[84:87]\n"
        "\tv_mul_u32_u24 v225, 0x2000, v224\n"
        "\tv_mfma_f32_16x16x32_bf16 v[88:91], v[96:99], v[148:151], v[88:91]\n"
        "\tv_lshl_or_b32 v252, v225, 16, v225\n"
        "\tv_mfma_f32_16x16x32_bf16 v[88:91], v[92:95], v[152:155], v[88:91]\n"
        "\tv_xor_b32 v253, 0xd600d600, v252\n"
        "\tv_mfma_f32_16x16x32_bf16 v[60:63], v[156:159], v[188:191], v[60:63]\n"
        "\tds_read_b128 v[124:127], v42 offset:320\n"
        "\tbuffer_load_dwordx4 v245, s[12:15], s96 offen offset:1024 lds\n"
        "\tv_mfma_f32_16x16x32_bf16 v[64:67], v[160:163], v[188:191], v[64:67]\n"
        "\tds_read_b64 v[92:93], v41 offset:3928\n"
        "\tv_lshlrev_b32 v224, 29, v224\n"
        "\tv_mfma_f32_16x16x32_bf16 v[68:71], v[164:167], v[188:191], v[68:71]\n"
        "\tds_read_b64 v[94:95], v41 offset:3936\n"
        "\tv_xor_b32 v254, 0x69800000, v224\n"
        "\tv_mfma_f32_16x16x32_bf16 v[72:75], v[168:171], v[188:191], v[72:75]\n"
        "\tds_read_b64 v[96:97], v41 offset:3416\n"
        "\ts_mov_b32 s29, 0x2a802a80\n"
        "\tv_mfma_f32_16x16x32_bf16 v[76:79], v[172:175], v[188:191], v[76:79]\n"
        "\tds_read_b64 v[98:99], v41 offset:3424\n"
        "\ts_waitcnt vmcnt(7)\n"
        "\tds_read_b64_tr_b16 a[64:65], v248 offset:16384\n"
        "\tbuffer_load_dwordx4 v246, s[12:15], s96 offen offset:2048 lds\n"
        "\tv_mfma_f32_16x16x32_bf16 v[80:83], v[176:179], v[188:191], v[80:83]\n"
        "\tds_read_b64_tr_b16 a[66:67], v248 offset:18432\n"
        "\tds_read_b64_tr_b16 a[68:69], v249 offset:16384\n"
        "\ts_mov_b32 s30, 0xe000e000\n"
        "\ts_waitcnt lgkmcnt(15)\n"
        "\tv_mfma_f32_16x16x32_bf16 v[84:87], v[180:183], v[188:191], v[84:87]\n"
        "\tds_read_b64 v[100:101], v41 offset:2904\n"
        "\tds_read_b64_tr_b16 a[70:71], v249 offset:18432\n"
        "\ts_mov_b32 s77, s3\n"
        "\tv_mfma_f32_16x16x32_bf16 v[88:91], v[184:187], v[188:191], v[88:91]\n"
        "\tds_read_b64 v[102:103], v41 offset:2912\n"
        "\tds_read_b64_tr_b16 a[72:73], v250 offset:16384\n"
        "\ts_waitcnt vmcnt(6)\n"
        "\tv_cmp_ne_u64 vcc, v[228:229], v[20:21]\n"
        "\ts_waitcnt lgkmcnt(15)\n"
        "\tv_mfma_f32_16x16x32_bf16 v[64:67], v[156:159], v[192:195], v[64:67]\n"
        "\tds_read_b64_tr_b16 a[74:75], v250 offset:18432\n"
        "\tds_read_b64_tr_b16 a[76:77], v251 offset:16384\n"
        "\tbuffer_load_dwordx4 v247, s[12:15], s96 offen offset:3072 lds\n"
        "\tv_mfma_f32_16x16x32_bf16 v[68:71], v[160:163], v[192:195], v[68:71]\n"
        "\tds_read_b64 v[104:105], v41 offset:2392\n"
        "\tds_read_b64_tr_b16 a[78:79], v251 offset:18432\n"
        "\ts_cbranch_vccnz LRfa\n"
        "LBfa:\n"
        "\ts_barrier\n"
        "\tv_mfma_f32_16x16x32_bf16 v[72:75], v[164:167], v[192:195], v[72:75]\n"
        "\tds_read_b64 v[106:107], v41 offset:2400\n"
        "\ts_add_u32 m0, s66, 0\n"
        "\tv_mfma_f32_16x16x32_bf16 v[76:79], v[168:171], v[192:195], v[76:79]\n"
        "\tds_read_b64 v[108:109], v41 offset:1880\n"
        "\ts_add_u32 s96, s39, 32768\n"
        "\tv_mfma_f32_16x16x32_bf16 v[80:83], v[172:175], v[192:195], v[80:83]\n"
        "\tds_read_b64 v[110:111], v41 offset:1888\n"
        "\tbuffer_load_dwordx4 v244, s[12:15], s96 offen offset:0 lds\n"
        "\tv_mfma_f32_16x16x32_bf16 v[84:87], v[176:179], v[192:195], v[84:87]\n"
        "\tds_read_b64 v[112:113], v41 offset:1368\n"
        "\ts_lshr_b32 s62, s77, 1\n"
        "\tv_mfma_f32_16x16x32_bf16 v[88:91], v[180:183], v[192:195], v[88:91]\n"
        "\tds_read_b64 v[114:115], v41 offset:1376\n"
        "\ts_xor_b32 s57, s77, 2\n"
        "\tv_mfma_f32_16x16x32_bf16 v[68:71], v[156:159], v[196:199], v[68:71]\n"
        "\tds_read_b64 v[116:117], v41 offset:856\n"
        "\ts_lshl_b32 s58, s43, 2\n"
        "\tv_mfma_f32_16x16x32_bf16 v[72:75], v[160:163], v[196:199], v[72:75]\n"
        "\tds_read_b64 v[118:119], v41 offset:864\n"
        "\tbuffer_load_dwordx4 v245, s[12:15], s96 offen offset:1024 lds\n"
        "\tv_mfma_f32_16x16x32_bf16 v[76:79], v[164:167], v[196:199], v[76:79]\n"
        "\tds_read_b64 v[120:121], v41 offset:344\n"
        "\ts_add_u32 s58, s58, s57\n"
        "\tv_mfma_f32_16x16x32_bf16 v[80:83], v[168:171], v[196:199], v[80:83]\n"
        "\tds_read_b64 v[122:123], v41 offset:352\n"
        "\ts_lshl_b32 s58, s58, 2\n"
        "\tv_mfma_f32_16x16x32_bf16 v[84:87], v[172:175], v[196:199], v[84:87]\n"
        "\tds_read_b128 v[128:131], v42 offset:832\n"
        "\ts_add_u32 s58, s58, s3\n"
        "\tv_mfma_f32_16x16x32_bf16 v[88:91], v[176:179], v[196:199], v[88:91]\n"
        "\tds_read_b128 v[132:135], v42 offset:1344\n"
        "\tbuffer_load_dwordx4 v246, s[12:15], s96 offen offset:2048 lds\n"
        "\tv_mfma_f32_16x16x32_bf16 v[72:75], v[156:159], v[200:203], v[72:75]\n"
        "\tds_read_b128 v[136:139], v42 offset:1856\n"
        "\ts_mul_i32 s58, s58, 12288\n"
        "\tv_mfma_f32_16x16x32_bf16 v[76:79], v[160:163], v[200:203], v[76:79]\n"
        "\tds_read_b128 v[140:143], v42 offset:2368\n"
        "\ts_add_u32 s58, s58, 0x500000\n"
        "\tv_mfma_f32_16x16x32_bf16 v[80:83], v[164:167], v[200:203], v[80:83]\n"
        "\ts_waitcnt vmcnt(7)\n"
        "\tds_read_b64_tr_b16 a[80:81], v248 offset:20480\n"
        "\tds_read_b64_tr_b16 a[82:83], v248 offset:22528\n"
        "\ts_add_u32 s40, s58, s24\n"
        "\tv_mfma_f32_16x16x32_bf16 v[84:87], v[168:171], v[200:203], v[84:87]\n"
        "\tds_read_b128 v[144:147], v42 offset:2880\n"
        "\tbuffer_load_dwordx4 v247, s[12:15], s96 offen offset:3072 lds\n"
        "\tv_mfma_f32_16x16x32_bf16 v[88:91], v[172:175], v[200:203], v[88:91]\n"
        "\tds_read_b128 v[148:151], v42 offset:3392\n"
        "\ts_add_u32 m0, s66, 4096\n"
        "\tv_mfma_f32_16x16x32_bf16 v[76:79], v[156:159], v[204:207], v[76:79]\n"
        "\tds_read_b64_tr_b16 a[84:85], v249 offset:20480\n"
        "\ts_add_u32 s96, s36, 65536\n"
        "\tv_mfma_f32_16x16x32_bf16 v[80:83], v[160:163], v[204:207], v[80:83]\n"
        "\tds_read_b128 v[152:155], v42 offset:3904\n"
        "\ts_add_u32 s45, s58, s25\n"
        "\tv_mfma_f32_16x16x32_bf16 v[84:87], v[164:167], v[204:207], v[84:87]\n"
        "\tds_read_b64_tr_b16 a[86:87], v249 offset:22528\n"
        "\tbuffer_load_dwordx4 v244, s[12:15], s96 offen offset:0 lds\n"
        "\tv_mfma_f32_16x16x32_bf16 v[88:91], v[168:171], v[204:207], v[88:91]\n"
        "\tds_read_b64_tr_b16 a[88:89], v250 offset:20480\n"
        "\ts_add_u32 s46, s58, s26\n"
        "\ts_waitcnt lgkmcnt(15)\n"
        "\tv_mfma_f32_16x16x32_bf16 v[80:83], v[156:159], v[208:211], v[80:83]\n"
        "\tds_read_b64_tr_b16 a[90:91], v250 offset:22528\n"
        "\ts_add_u32 s48, s58, s27\n"
        "\tv_mfma_f32_16x16x32_bf16 v[84:87], v[160:163], v[208:211], v[84:87]\n"
        "\tds_read_b64_tr_b16 a[92:93], v251 offset:20480\n"
        "\ts_xor_b32 s58, s3, 2\n"
        "\tv_mfma_f32_16x16x32_bf16 v[88:91], v[164:167], v[208:211], v[88:91]\n"
        "\tds_read_b64_tr_b16 a[94:95], v251 offset:22528\n"
        "\tbuffer_load_dwordx4 v245, s[12:15], s96 offen offset:1024 lds\n"
        "\tv_mfma_f32_16x16x32_bf16 v[84:87], v[156:159], v[212:215], v[84:87]\n"
        "\ts_mul_i32 s58, s58, 12288\n"
        "\tv_mfma_f32_16x16x32_bf16 v[88:91], v[160:163], v[212:215], v[88:91]\n"
        "\ts_add_u32 s58, s58, 0\n"
        "\tv_mfma_f32_16x16x32_bf16 v[88:91], v[156:159], v[216:219], v[88:91]\n"
        "\tv_add_u32 v14, s58, v1\n"
        "\tv_mfma_f32_16x16x32_bf16 v[60:63], v[92:95], v[124:127], v[60:63]\n"
        "\tds_read_b128 v[188:191], v42 offset:256\n"
        "\tbuffer_load_dwordx4 v246, s[12:15], s96 offen offset:2048 lds\n"
        "\tv_mfma_f32_16x16x32_bf16 v[64:67], v[96:99], v[124:127], v[64:67]\n"
        "\tds_read_b64 v[156:157], v41 offset:3864\n"
        "\ts_lshr_b32 s57, s57, 1\n"
        "\tv_mfma_f32_16x16x32_bf16 v[68:71], v[100:103], v[124:127], v[68:71]\n"
        "\tds_read_b64 v[158:159], v41 offset:3872\n"
        "\ts_lshl_b32 s58, s43, 1\n"
        "\tv_mfma_f32_16x16x32_bf16 v[72:75], v[104:107], v[124:127], v[72:75]\n"
        "\tds_read_b64 v[160:161], v41 offset:3352\n"
        "\ts_add_u32 s58, s58, s57\n"
        "\tv_mfma_f32_16x16x32_bf16 v[76:79], v[108:111], v[124:127], v[76:79]\n"
        "\tds_read_b64 v[162:163], v41 offset:3360\n"
        "\tbuffer_load_dwordx4 v247, s[12:15], s96 offen offset:3072 lds\n"
        "\tv_mfma_f32_16x16x32_bf16 v[80:83], v[112:115], v[124:127], v[80:83]\n"
        "\ts_waitcnt vmcnt(8)\n"
        "\tds_read_b64_tr_b16 a[96:97], v248 offset:24576\n"
        "\ts_add_u32 m0, s66, 8192\n"
        "\ts_waitcnt lgkmcnt(15)\n"
        "\tv_mfma_f32_16x16x32_bf16 v[84:87], v[116:119], v[124:127], v[84:87]\n"
        "\tds_read_b64 v[164:165], v41 offset:2840\n"
        "\ts_add_u32 s96, s37, 65536\n"
        "\tv_mfma_f32_16x16x32_bf16 v[88:91], v[120:123], v[124:127], v[88:91]\n"
        "\tds_read_b64 v[166:167], v41 offset:2848\n"
        "\ts_lshl_b32 s58, s58, 2\n"
        "\tv_mfma_f32_16x16x32_bf16 v[64:67], v[92:95], v[128:131], v[64:67]\n"
        "\tds_read_b64_tr_b16 a[98:99], v248 offset:26624\n"
        "\tbuffer_load_dwordx4 v244, s[12:15], s96 offen offset:0 lds\n"
        "\tv_mfma_f32_16x16x32_bf16 v[68:71], v[96:99], v[128:131], v[68:71]\n"
        "\tds_read_b64 v[168:169], v41 offset:2328\n"
        "\ts_add_u32 s58, s58, s3\n"
        "\tv_mfma_f32_16x16x32_bf16 v[72:75], v[100:103], v[128:131], v[72:75]\n"
        "\tds_read_b64 v[170:171], v41 offset:2336\n"
        "\ts_lshl_b32 s58, s58, 12\n"
        "\tv_mfma_f32_16x16x32_bf16 v[76:79], v[104:107], v[128:131], v[76:79]\n"
        "\tds_read_b64_tr_b16 a[100:101], v249 offset:24576\n"
        "\ts_add_u32 s78, s58, 0xb00000\n"
        "\tv_mfma_f32_16x16x32_bf16 v[80:83], v[108:111], v[128:131], v[80:83]\n"
        "\tds_read_b64 v[172:173], v41 offset:1816\n"
        "\tbuffer_load_dwordx4 v245, s[12:15], s96 offen offset:1024 lds\n"
        "\tv_mfma_f32_16x16x32_bf16 v[84:87], v[112:115], v[128:131], v[84:87]\n"
        "\tds_read_b64 v[174:175], v41 offset:1824\n"
        "\ts_xor_b32 s57, s77, 3\n"
        "\tv_mfma_f32_16x16x32_bf16 v[88:91], v[116:119], v[128:131], v[88:91]\n"
        "\tds_read_b64_tr_b16 a[102:103], v249 offset:26624\n"
        "\ts_lshl_b32 s58, s43, 2\n"
        "\tv_mfma_f32_16x16x32_bf16 v[68:71], v[92:95], v[132:135], v[68:71]\n"
        "\tds_read_b64 v[176:177], v41 offset:1304\n"
        "\ts_add_u32 s58, s58, s57\n"
        "\tv_mfma_f32_16x16x32_bf16 v[72:75], v[96:99], v[132:135], v[72:75]\n"
        "\tds_read_b64 v[178:179], v41 offset:1312\n"
        "\tbuffer_load_dwordx4 v246, s[12:15], s96 offen offset:2048 lds\n"
        "\tv_mfma_f32_16x16x32_bf16 v[76:79], v[100:103], v[132:135], v[76:79]\n"
        "\tds_read_b64_tr_b16 a[104:105], v250 offset:24576\n"
        "\ts_lshl_b32 s58, s58, 2\n"
        "\tv_mfma_f32_16x16x32_bf16 v[80:83], v[104:107], v[132:135], v[80:83]\n"
        "\tds_read_b64 v[180:181], v41 offset:792\n"
        "\ts_add_u32 s58, s58, s3\n"
        "\tv_mfma_f32_16x16x32_bf16 v[84:87], v[108:111], v[132:135], v[84:87]\n"
        "\tds_read_b64 v[182:183], v41 offset:800\n"
        "\ts_mul_i32 s58, s58, 12288\n"
        "\tv_mfma_f32_16x16x32_bf16 v[88:91], v[112:115], v[132:135], v[88:91]\n"
        "\tds_read_b64_tr_b16 a[106:107], v250 offset:26624\n"
        "\tbuffer_load_dwordx4 v247, s[12:15], s96 offen offset:3072 lds\n"
        "\tv_mfma_f32_16x16x32_bf16 v[72:75], v[92:95], v[136:139], v[72:75]\n"
        "\tds_read_b64 v[184:185], v41 offset:280\n"
        "\ts_add_u32 m0, s66, 12288\n"
        "\tv_mfma_f32_16x16x32_bf16 v[76:79], v[96:99], v[136:139], v[76:79]\n"
        "\tds_read_b64 v[186:187], v41 offset:288\n"
        "\ts_add_u32 s96, s80, 12288\n"
        "\tv_mfma_f32_16x16x32_bf16 v[80:83], v[100:103], v[136:139], v[80:83]\n"
        "\tds_read_b64_tr_b16 a[108:109], v251 offset:24576\n"
        "\ts_add_u32 s58, s58, 0x500000\n"
        "\tv_mfma_f32_16x16x32_bf16 v[84:87], v[104:107], v[136:139], v[84:87]\n"
        "\tds_read_b128 v[192:195], v42 offset:768\n"
        "\tbuffer_load_dwordx4 v1, s[8:11], s96 offen offset:0 sc1 lds\n"
        "\tv_mfma_f32_16x16x32_bf16 v[88:91], v[108:111], v[136:139], v[88:91]\n"
        "\tds_read_b128 v[196:199], v42 offset:1280\n"
        "\ts_add_u32 s49, s58, s24\n"
        "\tv_mfma_f32_16x16x32_bf16 v[76:79], v[92:95], v[140:143], v[76:79]\n"
        "\tds_read_b64_tr_b16 a[110:111], v251 offset:26624\n"
        "\ts_add_u32 s50, s58, s25\n"
        "\tv_mfma_f32_16x16x32_bf16 v[80:83], v[96:99], v[140:143], v[80:83]\n"
        "\tds_read_b128 v[200:203], v42 offset:1792\n"
        "\ts_add_u32 s51, s58, s26\n"
        "\tv_mfma_f32_16x16x32_bf16 v[84:87], v[100:103], v[140:143], v[84:87]\n"
        "\tds_read_b128 v[204:207], v42 offset:2304\n"
        "\tbuffer_load_dwordx4 v1, s[8:11], s96 offen offset:1024 sc1 lds\n"
        "\tv_mfma_f32_16x16x32_bf16 v[88:91], v[104:107], v[140:143], v[88:91]\n"
        "\ts_waitcnt vmcnt(10)\n"
        "\tds_read_b64_tr_b16 a[112:113], v248 offset:0\n"
        "\ts_add_u32 s63, s58, s27\n"
        "\ts_waitcnt lgkmcnt(15)\n"
        "\tv_mfma_f32_16x16x32_bf16 v[80:83], v[92:95], v[144:147], v[80:83]\n"
        "\tds_read_b128 v[208:211], v42 offset:2816\n"
        "\ts_xor_b32 s58, s3, 3\n"
        "\tv_mfma_f32_16x16x32_bf16 v[84:87], v[96:99], v[144:147], v[84:87]\n"
        "\tds_read_b128 v[212:215], v42 offset:3328\n"
        "\ts_mul_i32 s58, s58, 12288\n"
        "\tv_mfma_f32_16x16x32_bf16 v[88:91], v[100:103], v[144:147], v[88:91]\n"
        "\tds_read_b64_tr_b16 a[114:115], v248 offset:2048\n"
        "\tbuffer_load_dwordx4 v1, s[8:11], s96 offen offset:2048 sc1 lds\n"
        "\tv_mfma_f32_16x16x32_bf16 v[84:87], v[92:95], v[148:151], v[84:87]\n"
        "\tds_read_b128 v[216:219], v42 offset:3840\n"
        "\ts_add_u32 s58, s58, 4096\n"
        "\tv_mfma_f32_16x16x32_bf16 v[88:91], v[96:99], v[148:151], v[88:91]\n"
        "\tds_read_b64_tr_b16 a[116:117], v249 offset:0\n"
        "\tv_add_u32 v15, s58, v1\n"
        "\tv_mfma_f32_16x16x32_bf16 v[88:91], v[92:95], v[152:155], v[88:91]\n"
        "\tds_read_b64_tr_b16 a[118:119], v249 offset:2048\n"
        "\ts_xor_b32 s57, s77, 1\n"
        "\tv_mfma_f32_16x16x32_bf16 v[60:63], v[156:159], v[188:191], v[60:63]\n"
        "\tds_read_b64_tr_b16 a[120:121], v250 offset:0\n"
        "\tbuffer_load_dwordx4 v1, s[8:11], s96 offen offset:3072 sc1 lds\n"
        "\tv_mfma_f32_16x16x32_bf16 v[64:67], v[160:163], v[188:191], v[64:67]\n"
        "\tds_read_b64_tr_b16 a[122:123], v250 offset:2048\n"
        "\ts_add_u32 m0, s66, 16384\n"
        "\tv_mfma_f32_16x16x32_bf16 v[68:71], v[164:167], v[188:191], v[68:71]\n"
        "\tds_read_b64_tr_b16 a[124:125], v251 offset:0\n"
        "\ts_add_u32 s96, s80, 16384\n"
        "\tv_mfma_f32_16x16x32_bf16 v[72:75], v[168:171], v[188:191], v[72:75]\n"
        "\ts_waitcnt lgkmcnt(0)\n"
        "\ts_barrier\n"
        "\tv_mfma_f32_16x16x32_bf16 v[76:79], v[172:175], v[188:191], v[76:79]\n"
        "\tds_read_b64_tr_b16 a[126:127], v251 offset:2048\n"
        "\tv_cvt_pk_bf16_f32 v60, v60, v61\n"
        "\tv_mfma_f32_16x16x32_bf16 v[80:83], v[176:179], v[188:191], v[80:83]\n"
        "\ts_waitcnt vmcnt(8)\n"
        "\tds_read_b64_tr_b16 a[128:129], v248 offset:4096\n"
        "\tv_cvt_pk_bf16_f32 v61, v62, v63\n"
        "\tv_mfma_f32_16x16x32_bf16 v[84:87], v[180:183], v[188:191], v[84:87]\n"
        "\tds_write_b64 v43, v[60:61] offset:0\n"
        "\tbuffer_load_dwordx4 v1, s[8:11], s96 offen offset:0 sc1 lds\n"
        "\tv_mfma_f32_16x16x32_bf16 v[88:91], v[184:187], v[188:191], v[88:91]\n"
        "\tds_read_b64_tr_b16 a[130:131], v248 offset:6144\n"
        "\ts_lshl_b32 s58, s43, 2\n"
        "\tv_mfma_f32_16x16x32_bf16 v[64:67], v[156:159], v[192:195], v[64:67]\n"
        "\tds_read_b64_tr_b16 a[132:133], v249 offset:4096\n"
        "\ts_add_u32 s58, s58, s57\n"
        "\tv_mfma_f32_16x16x32_bf16 v[68:71], v[160:163], v[192:195], v[68:71]\n"
        "\tds_read_b64_tr_b16 a[134:135], v249 offset:6144\n"
        "\ts_lshl_b32 s58, s58, 2\n"
        "\tv_mfma_f32_16x16x32_bf16 v[72:75], v[164:167], v[192:195], v[72:75]\n"
        "\tds_read_b64_tr_b16 a[136:137], v250 offset:4096\n"
        "\tbuffer_load_dwordx4 v1, s[8:11], s96 offen offset:1024 sc1 lds\n"
        "\tv_mfma_f32_16x16x32_bf16 v[76:79], v[168:171], v[192:195], v[76:79]\n"
        "\tds_read_b64_tr_b16 a[138:139], v250 offset:6144\n"
        "\ts_add_u32 s58, s58, s3\n"
        "\tv_mfma_f32_16x16x32_bf16 v[80:83], v[172:175], v[192:195], v[80:83]\n"
        "\tds_read_b64_tr_b16 a[140:141], v251 offset:4096\n"
        "\tv_cvt_pk_bf16_f32 v64, v64, v65\n"
        "\tv_mfma_f32_16x16x32_bf16 v[84:87], v[176:179], v[192:195], v[84:87]\n"
        "\tds_read_b64_tr_b16 a[142:143], v251 offset:6144\n"
        "\tv_cvt_pk_bf16_f32 v65, v66, v67\n"
        "\tv_mfma_f32_16x16x32_bf16 v[88:91], v[180:183], v[192:195], v[88:91]\n"
        "\tds_write_b64 v43, v[64:65] offset:512\n"
        "\tbuffer_load_dwordx4 v1, s[8:11], s96 offen offset:2048 sc1 lds\n"
        "\tv_mfma_f32_16x16x32_bf16 v[68:71], v[156:159], v[196:199], v[68:71]\n"
        "\ts_waitcnt vmcnt(7)\n"
        "\tds_read_b64_tr_b16 a[144:145], v248 offset:8192\n"
        "\ts_mul_i32 s58, s58, 12288\n"
        "\tv_mfma_f32_16x16x32_bf16 v[72:75], v[160:163], v[196:199], v[72:75]\n"
        "\tds_read_b64_tr_b16 a[146:147], v248 offset:10240\n"
        "\ts_add_u32 s58, s58, 0x500000\n"
        "\tv_mfma_f32_16x16x32_bf16 v[76:79], v[164:167], v[196:199], v[76:79]\n"
        "\tds_read_b64_tr_b16 a[148:149], v249 offset:8192\n"
        "\ts_movk_i32 s35, 24576\n"
        "\tv_mfma_f32_16x16x32_bf16 v[80:83], v[168:171], v[196:199], v[80:83]\n"
        "\tds_read_b64_tr_b16 a[150:151], v249 offset:10240\n"
        "\tbuffer_load_dwordx2 v[232:233], v230, s[8:11], s35 offen sc1\n"
        "\tv_mfma_f32_16x16x32_bf16 v[84:87], v[172:175], v[196:199], v[84:87]\n"
        "\tds_read_b64_tr_b16 a[152:153], v250 offset:8192\n"
        "\tv_cvt_pk_bf16_f32 v68, v68, v69\n"
        "\tv_mfma_f32_16x16x32_bf16 v[88:91], v[176:179], v[196:199], v[88:91]\n"
        "\tds_read_b64_tr_b16 a[154:155], v250 offset:10240\n"
        "\tv_cvt_pk_bf16_f32 v69, v70, v71\n"
        "\tv_mfma_f32_16x16x32_bf16 v[72:75], v[156:159], v[200:203], v[72:75]\n"
        "\tds_write_b64 v43, v[68:69] offset:1024\n"
        "\ts_add_u32 s65, s58, s24\n"
        "\tv_mfma_f32_16x16x32_bf16 v[76:79], v[160:163], v[200:203], v[76:79]\n"
        "\tds_read_b64_tr_b16 a[156:157], v251 offset:8192\n"
        "\tbuffer_load_dwordx4 v1, s[8:11], s96 offen offset:3072 sc1 lds\n"
        "\tv_mfma_f32_16x16x32_bf16 v[80:83], v[164:167], v[200:203], v[80:83]\n"
        "\tds_read_b64_tr_b16 a[158:159], v251 offset:10240\n"
        "\ts_add_u32 m0, s66, 20480\n"
        "\tv_mfma_f32_16x16x32_bf16 v[84:87], v[168:171], v[200:203], v[84:87]\n"
        "\ts_add_u32 s96, s80, 20480\n"
        "\tv_mfma_f32_16x16x32_bf16 v[88:91], v[172:175], v[200:203], v[88:91]\n"
        "\tv_cvt_pk_bf16_f32 v72, v72, v73\n"
        "\tv_mfma_f32_16x16x32_bf16 v[76:79], v[156:159], v[204:207], v[76:79]\n"
        "\tv_cvt_pk_bf16_f32 v73, v74, v75\n"
        "\tv_mfma_f32_16x16x32_bf16 v[80:83], v[160:163], v[204:207], v[80:83]\n"
        "\tds_write_b64 v43, v[72:73] offset:1536\n"
        "\tbuffer_load_dwordx4 v1, s[8:11], s96 offen offset:0 sc1 lds\n"
        "\tv_mfma_f32_16x16x32_bf16 v[84:87], v[164:167], v[204:207], v[84:87]\n"
        "\ts_add_u32 s68, s58, s25\n"
        "\tv_mfma_f32_16x16x32_bf16 v[88:91], v[168:171], v[204:207], v[88:91]\n"
        "\ts_add_u32 s69, s58, s26\n"
        "\tv_mfma_f32_16x16x32_bf16 v[80:83], v[156:159], v[208:211], v[80:83]\n"
        "\tv_cvt_pk_bf16_f32 v76, v76, v77\n"
        "\tv_mfma_f32_16x16x32_bf16 v[84:87], v[160:163], v[208:211], v[84:87]\n"
        "\tv_cvt_pk_bf16_f32 v77, v78, v79\n"
        "\tv_mfma_f32_16x16x32_bf16 v[88:91], v[164:167], v[208:211], v[88:91]\n"
        "\tds_write_b64 v43, v[76:77] offset:2048\n"
        "\tbuffer_load_dwordx4 v1, s[8:11], s96 offen offset:1024 sc1 lds\n"
        "\tv_mfma_f32_16x16x32_bf16 v[84:87], v[156:159], v[212:215], v[84:87]\n"
        "\ts_add_u32 s97, s58, s27\n"
        "\tv_mfma_f32_16x16x32_bf16 v[88:91], v[160:163], v[212:215], v[88:91]\n"
        "\tv_cvt_pk_bf16_f32 v80, v80, v81\n"
        "\tv_mfma_f32_16x16x32_bf16 v[88:91], v[156:159], v[216:219], v[88:91]\n"
        "\tv_cvt_pk_bf16_f32 v81, v82, v83\n"
        "\tds_write_b64 v43, v[80:81] offset:2560\n"
        "\ts_nop 1\n"
        "\tv_cvt_pk_bf16_f32 v84, v84, v85\n"
        "\tv_cvt_pk_bf16_f32 v85, v86, v87\n"
        "\tds_write_b64 v43, v[84:85] offset:3072\n"
        "\ts_nop 0\n"
        "\tv_cvt_pk_bf16_f32 v88, v88, v89\n"
        "\tv_cvt_pk_bf16_f32 v89, v90, v91\n"
        "\tds_write_b64 v43, v[88:89] offset:3584\n"
        "\ts_waitcnt lgkmcnt(0)\n"
        "\ts_barrier\n"
        "\tds_read_b128 v[92:95], v31 offset:0\n"
        "\tds_read_b128 v[96:99], v31 offset:4096\n"
        "\tds_read_b128 v[100:103], v31 offset:8192\n"
        "\tds_read_b128 v[104:107], v31 offset:12288\n"
        "\tbuffer_load_dwordx4 v1, s[8:11], s96 offen offset:2048 sc1 lds\n"
        "\tbuffer_load_dwordx4 v1, s[8:11], s96 offen offset:3072 sc1 lds\n"
        "\ts_waitcnt lgkmcnt(3)\n"
        "\tv_dot2_f32_bf16 v44, v92, s88, v44\n"
        "\tv_dot2_f32_bf16 v45, v92, s89, v45\n"
        "\tv_dot2_f32_bf16 v46, v93, s88, v46\n"
        "\tv_dot2_f32_bf16 v47, v93, s89, v47\n"
        "\tv_dot2_f32_bf16 v48, v94, s88, v48\n"
        "\tv_dot2_f32_bf16 v49, v94, s89, v49\n"
        "\tv_dot2_f32_bf16 v50, v95, s88, v50\n"
        "\tv_dot2_f32_bf16 v51, v95, s89, v51\n"
        "\ts_waitcnt lgkmcnt(2)\n"
        "\tv_dot2_f32_bf16 v44, v96, s88, v44\n"
        "\tv_dot2_f32_bf16 v45, v96, s89, v45\n"
        "\tv_dot2_f32_bf16 v46, v97, s88, v46\n"
        "\tv_dot2_f32_bf16 v47, v97, s89, v47\n"
        "\tv_dot2_f32_bf16 v48, v98, s88, v48\n"
        "\tv_dot2_f32_bf16 v49, v98, s89, v49\n"
        "\tv_dot2_f32_bf16 v50, v99, s88, v50\n"
        "\tv_dot2_f32_bf16 v51, v99, s89, v51\n"
        "\tv_cvt_pk_bf16_f32 v108, v44, v45\n"
        "\tv_cvt_pk_bf16_f32 v109, v46, v47\n"
        "\tv_cvt_pk_bf16_f32 v110, v48, v49\n"
        "\tv_cvt_pk_bf16_f32 v111, v50, v51\n"
        "\tbuffer_store_dwordx4 v[108:111], v25, s[8:11], s47 offen offset:0 sc1\n"
        "\ts_waitcnt lgkmcnt(1)\n"
        "\tv_dot2_f32_bf16 v52, v100, s88, v52\n"
        "\tv_dot2_f32_bf16 v53, v100, s89, v53\n"
        "\tv_dot2_f32_bf16 v54, v101, s88, v54\n"
        "\tv_dot2_f32_bf16 v55, v101, s89, v55\n"
        "\tv_dot2_f32_bf16 v56, v102, s88, v56\n"
        "\tv_dot2_f32_bf16 v57, v102, s89, v57\n"
        "\tv_dot2_f32_bf16 v58, v103, s88, v58\n"
        "\tv_dot2_f32_bf16 v59, v103, s89, v59\n"
        "\ts_waitcnt lgkmcnt(0)\n"
        "\tv_dot2_f32_bf16 v52, v104, s88, v52\n"
        "\tv_dot2_f32_bf16 v53, v104, s89, v53\n"
        "\tv_dot2_f32_bf16 v54, v105, s88, v54\n"
        "\tv_dot2_f32_bf16 v55, v105, s89, v55\n"
        "\tv_dot2_f32_bf16 v56, v106, s88, v56\n"
        "\tv_dot2_f32_bf16 v57, v106, s89, v57\n"
        "\tv_dot2_f32_bf16 v58, v107, s88, v58\n"
        "\tv_dot2_f32_bf16 v59, v107, s89, v59\n"
        "\tv_cvt_pk_bf16_f32 v112, v52, v53\n"
        "\tv_cvt_pk_bf16_f32 v113, v54, v55\n"
        "\tv_cvt_pk_bf16_f32 v114, v56, v57\n"
        "\tv_cvt_pk_bf16_f32 v115, v58, v59\n"
        "\tbuffer_store_dwordx4 v[112:115], v25, s[8:11], s47 offen offset:64 sc1\n"
        "\ts_memrealtime s[94:95]\n"
        "\ts_waitcnt vmcnt(7)\n"
        "\tv_xor_b32 v234, v232, v20\n"
        "\tv_xor_b32 v235, v233, v21\n"
        "\tv_and_or_b32 v234, v234, s83, v235\n"
        "\tv_cmp_ne_u32 vcc, 0, v234\n"
        "\ts_cbranch_vccnz LRf\n"
        "LBf:\n"
        "\tv_subrev_u32 v234, s32, v232\n"
        "\tv_bfe_i32 v234, v234, 0, 16\n"
        "\ts_nop 1\n"
        "\tv_max_i32_dpp v234, v234, v234 row_shr:1 row_mask:0xf bank_mask:0xf\n"
        "\ts_nop 1\n"
        "\tv_max_i32_dpp v234, v234, v234 row_shr:2 row_mask:0xf bank_mask:0xf\n"
        "\ts_nop 1\n"
        "\tv_max_i32_dpp v234, v234, v234 row_shr:4 row_mask:0xf bank_mask:0xf\n"
        "\ts_nop 1\n"
        "\tv_max_i32_dpp v234, v234, v234 row_shr:8 row_mask:0xf bank_mask:0xf\n"
        "\ts_nop 1\n"
        "\tv_max_i32_dpp v234, v234, v234 row_bcast:15 row_mask:0xa bank_mask:0xf\n"
        "\ts_nop 1\n"
        "\tv_max_i32_dpp v234, v234, v234 row_bcast:31 row_mask:0xc bank_mask:0xf\n"
        "\ts_nop 0\n"
        "\tv_readlane_b32 s34, v234, 63\n"
        "\ts_lshl_b32 s35, s3, 2\n"
        "\ts_add_u32 s35, s35, 158736\n"
        "\tv_mov_b32 v235, s35\n"
        "\tv_mov_b32 v234, s34\n"
        "\tds_write_b32 v235, v234\n"
        "\ts_waitcnt lgkmcnt(0)\n"
        "\ts_barrier\n"
        "\tv_mov_b32 v92, 158736\n"
        "\ts_add_u32 s33, s94, 40\n"
        "\tds_read_b128 v[92:95], v92\n"
        "\ts_add_u32 m0, s66, 24576\n"
        "\ts_add_u32 s96, s80, 24576\n"
        "\tbuffer_load_dwordx4 v1, s[8:11], s96 offen offset:0 sc1 lds\n"
        "\ts_xor_b32 s58, s3, 1\n"
        "\ts_mul_i32 s58, s58, 12288\n"
        "\ts_add_u32 s58, s58, 8192\n"
        "\tv_add_u32 v16, s58, v1\n"
        "\ts_lshr_b32 s57, s57, 1\n"
        "\ts_lshl_b32 s58, s43, 1\n"
        "\tbuffer_load_dwordx4 v1, s[8:11], s96 offen offset:1024 sc1 lds\n"
        "\ts_add_u32 s58, s58, s57\n"
        "\ts_lshl_b32 s58, s58, 2\n"
        "\ts_add_u32 s58, s58, s3\n"
        "\ts_lshl_b32 s58, s58, 12\n"
        "\ts_add_u32 s79, s58, 0xb00000\n"
        "\ts_xor_b32 s57, s77, 0\n"
        "\tbuffer_load_dwordx4 v1, s[8:11], s96 offen offset:2048 sc1 lds\n"
        "\ts_lshl_b32 s58, s43, 2\n"
        "\ts_add_u32 s58, s58, s57\n"
        "\ts_lshl_b32 s58, s58, 2\n"
        "\ts_add_u32 s58, s58, s3\n"
        "\ts_mul_i32 s58, s58, 12288\n"
        "\ts_add_u32 s58, s58, 0x500000\n"
        "\tbuffer_load_dwordx4 v1, s[8:11], s96 offen offset:3072 sc1 lds\n"
        "\ts_add_u32 s98, s58, s24\n"
        "\ts_add_u32 s99, s58, s25\n"
        "\ts_add_u32 s74, s58, s26\n"
        "\ts_add_u32 s76, s58, s27\n"
        "\ts_add_u32 s57, s67, 10240\n"
        "\tv_lshl_add_u32 v10, v27, 2, s57\n"
        "\ts_add_u32 m0, s66, 0\n"
        "\ts_add_u32 s96, s80, 0\n"
        "\tbuffer_load_dwordx4 v1, s[8:11], s96 offen offset:0 sc1 lds\n"
        "\tv_lshrrev_b32 v225, 2, v27\n"
        "\tv_lshl_add_u32 v225, v225, 4, v27\n"
        "\tv_lshlrev_b32 v9, 2, v225\n"
        "\ts_and_b32 s57, s43, 1\n"
        "\ts_lshl_b32 s57, s57, 1\n"
        "\ts_add_u32 s57, s57, s62\n"
        "\tbuffer_load_dwordx4 v1, s[8:11], s96 offen offset:1024 sc1 lds\n"
        "\ts_mul_i32 s57, s57, 12288\n"
        "\ts_and_b32 s58, s3, 1\n"
        "\ts_lshl_b32 s59, s58, 6\n"
        "\ts_add_u32 s57, s57, s59\n"
        "\ts_add_u32 s57, s57, 124928\n"
        "\tv_lshl_add_u32 v11, v27, 2, s57\n"
        "\tbuffer_load_dwordx4 v1, s[8:11], s96 offen offset:2048 sc1 lds\n"
        "\tv_lshrrev_b32 v224, 1, v26\n"
        "\tv_and_b32 v225, 16, v1\n"
        "\tv_lshl_add_u32 v225, v224, 6, v225\n"
        "\ts_lshl_b32 s58, s58, 5\n"
        "\tv_add_u32 v12, s58, v225\n"
        "\tv_lshl_add_u32 v225, v29, 5, v30\n"
        "\tbuffer_load_dwordx4 v1, s[8:11], s96 offen offset:3072 sc1 lds\n"
        "\ts_add_u32 s57, s67, 8192\n"
        "\tv_add_u32 v13, s57, v225\n"
        "\ts_mul_i32 s57, s3, 12288\n"
        "\tv_add_u32 v17, s57, v1\n"
        "\tv_lshrrev_b32 v224, 3, v26\n"
        "\tv_and_b32 v225, 0x70, v1\n"
        "\ts_add_u32 m0, s66, 4096\n"
        "\ts_add_u32 s96, s80, 4096\n"
        "\tbuffer_load_dwordx4 v1, s[8:11], s96 offen offset:0 sc1 lds\n"
        "\tv_lshl_add_u32 v18, v224, 10, v225\n"
        "\ts_mul_i32 s57, s42, 7168\n"
        "\ts_lshl_b32 s58, s3, 11\n"
        "\ts_add_u32 s57, s57, s58\n"
        "\ts_add_u32 s81, s57, 0xdc0000\n"
        "\tv_add_u32 v8, 122880, v1\n"
        "\tbuffer_load_dwordx4 v1, s[8:11], s96 offen offset:1024 sc1 lds\n"
        "\ts_add_u32 s57, s3, 1\n"
        "\ts_lshr_b32 s57, s57, 2\n"
        "\ts_lshl_b32 s57, s57, 30\n"
        "\tv_or_b32 v7, s57, v1\n"
        "\tbuffer_load_dwordx4 v1, s[8:11], s96 offen offset:2048 sc1 lds\n"
        "\tbuffer_load_dwordx4 v1, s[8:11], s96 offen offset:3072 sc1 lds\n"
        "\ts_add_u32 m0, s66, 8192\n"
        "\ts_add_u32 s96, s80, 8192\n"
        "\tbuffer_load_dwordx4 v1, s[8:11], s96 offen offset:0 sc1 lds\n"
        "\tbuffer_load_dwordx4 v1, s[8:11], s96 offen offset:1024 sc1 lds\n"
        "\tbuffer_load_dwordx4 v1, s[8:11], s96 offen offset:2048 sc1 lds\n"
        "\tbuffer_load_dwordx4 v1, s[8:11], s96 offen offset:3072 sc1 lds\n"
        "\ts_sub_u32 s57, s67, s66\n"
        "\tv_add_u32 v248, s57, v248\n"
        "\tv_add_u32 v249, s57, v249\n"
        "\tv_add_u32 v250, s57, v250\n"
        "\tv_add_u32 v251, s57, v251\n"
        "\ts_waitcnt lgkmcnt(0)\n"
        "\tv_max3_i32 v92, v92, v93, v94\n"
        "\tv_max_i32 v92, v92, v95\n"
        "\ts_nop 0\n"
        "\tv_readfirstlane_b32 s31, v92\n"
        "\ts_nop 0\n"
        "\ts_add_u32 s31, s31, s33\n"
        "LWh:\n"
        "\ts_memrealtime s[34:35]\n"
        "\ts_waitcnt lgkmcnt(0)\n"
        "\ts_sub_u32 s35, s31, s34\n"
        "\ts_sub_u32 s35, s35, 1\n"
        "\ts_cmp_lt_u32 s35, 400\n"
        "\ts_cbranch_scc1 LWh\n"
        "\ts_add_u32 m0, s67, 0\n"
        "\ts_add_u32 s96, s73, 0\n"
        "\tbuffer_load_dwordx4 v2, s[8:11], s96 offen sc1 lds\n"
        "\tbuffer_load_dwordx4 v2, s[8:11], s96 offen offset:1024 sc1 lds\n"
        "\ts_add_u32 m0, s67, 2048\n"
        "\ts_add_u32 s96, s73, 2048\n"
        "\tbuffer_load_dwordx4 v2, s[8:11], s96 offen sc1 lds\n"
        "\tbuffer_load_dwordx4 v2, s[8:11], s96 offen offset:1024 sc1 lds\n"
        "\ts_mov_b32 s32, s34\n"
        "\ts_lshl_b32 s57, s41, 4\n"
        "\ts_add_u32 s58, s57, s42\n"
        "\ts_lshl_b32 s58, s58, 3\n"
        "\ts_add_u32 s58, s58, 26624\n"
        "\ts_lshl_b32 s57, s57, 3\n"
        "\ts_add_u32 s33, s57, 26624\n"
        "\ts_mov_b32 s57, 0xffff\n"
        "\tv_mov_b32 v240, s32\n"
        "\tv_bfi_b32 v240, s57, v240, v20\n"
        "\tv_mov_b32 v241, v21\n"
        "\tbuffer_store_dwordx2 v[240:241], v19, s[8:11], s58 offen sc1\n"
        "\tv_bfe_u32 v242, v1, 4, 4\n"
        "\tv_lshlrev_b32 v242, 3, v242\n"
        "\ts_waitcnt vmcnt(5)\n"
        "\ts_barrier\n"
        "\tds_read_b128 v[208:211], v1 offset:0\n"
        "\tds_read_b128 v[212:215], v1 offset:1024\n"
        "\tds_read_b128 v[216:219], v1 offset:2048\n"
        "\tds_read_b128 v[220:223], v1 offset:3072\n"
        "\ts_add_u32 m0, s67, 4096\n"
        "\ts_add_u32 s96, s73, 4096\n"
        "\tbuffer_load_dwordx4 v2, s[8:11], s96 offen sc1 lds\n"
        "\tbuffer_load_dwordx4 v2, s[8:11], s96 offen offset:1024 sc1 lds\n"
        "\ts_add_u32 m0, s67, 6144\n"
        "\ts_add_u32 s96, s73, 6144\n"
        "\tbuffer_load_dwordx4 v2, s[8:11], s96 offen sc1 lds\n"
        "\tbuffer_load_dwordx4 v2, s[8:11], s96 offen offset:1024 sc1 lds\n"
        "\ts_add_u32 m0, s67, 8192\n"
        "\ts_add_u32 s96, s73, 8192\n"
        "\tbuffer_load_dwordx4 v2, s[8:11], s96 offen sc1 lds\n"
        "\tbuffer_load_dwordx4 v2, s[8:11], s96 offen offset:1024 sc1 lds\n"
        "\ts_add_u32 m0, s67, 10240\n"
        "\ts_add_u32 s96, s73, 10240\n"
        "\tbuffer_load_dwordx4 v2, s[8:11], s96 offen sc1 lds\n"
        "\tbuffer_load_dwordx4 v2, s[8:11], s96 offen offset:1024 sc1 lds\n"
        "\ts_waitcnt vmcnt(11)\n"
        "\tds_read_b64_tr_b16 v[24:25], v3 offset:0\n"
        "\tds_read_b64_tr_b16 v[26:27], v3 offset:1024\n"
        "\tds_read_b64_tr_b16 v[28:29], v4 offset:0\n"
        "\tds_read_b64_tr_b16 v[30:31], v4 offset:1024\n"
        "\ts_waitcnt vmcnt(9)\n"
        "\tds_read_b64_tr_b16 v[32:33], v3 offset:2048\n"
        "\tds_read_b64_tr_b16 v[34:35], v3 offset:3072\n"
        "\tds_read_b64_tr_b16 v[36:37], v4 offset:2048\n"
        "\tds_read_b64_tr_b16 v[38:39], v4 offset:3072\n"
        "\ts_waitcnt lgkmcnt(6)\n"
        "\tv_mfma_f32_16x16x32_bf16 v[184:187], a[224:227], v[24:27], 0\n"
        "\ts_waitcnt lgkmcnt(4)\n"
        "\tv_mfma_f32_16x16x32_bf16 v[188:191], a[224:227], v[28:31], 0\n"
        "\ts_add_u32 m0, s67, 0\n"
        "\ts_add_u32 s96, s73, 12288\n"
        "\tbuffer_load_dwordx4 v2, s[8:11], s96 offen sc1 lds\n"
        "\tbuffer_load_dwordx4 v2, s[8:11], s96 offen offset:1024 sc1 lds\n"
        "\ts_nop 3\n"
        "\tv_cmp_u_f32 vcc, v184, v188\n"
        "\ts_waitcnt lgkmcnt(0)\n"
        "\ts_add_u32 m0, s67, 2048\n"
        "\ts_add_u32 s96, s73, 14336\n"
        "\tbuffer_load_dwordx4 v2, s[8:11], s96 offen sc1 lds\n"
        "\tbuffer_load_dwordx4 v2, s[8:11], s96 offen offset:1024 sc1 lds\n"
        "\ts_cbranch_vccnz LRa0\n"
        "LBa0:\n"
        "\ts_waitcnt vmcnt(10)\n"
        "\tv_mfma_f32_16x16x32_bf16 v[192:195], a[224:227], v[32:35], v[184:187]\n"
        "\tds_read_b64_tr_b16 v[40:41], v3 offset:4096\n"
        "\tv_mfma_f32_16x16x32_bf16 v[196:199], a[224:227], v[36:39], v[188:191]\n"
        "\tds_read_b64_tr_b16 v[42:43], v3 offset:5120\n"
        "\tv_mfma_f32_16x16x32_bf16 a[244:247], v[24:27], v[24:27], 0\n"
        "\tds_read_b64_tr_b16 v[44:45], v4 offset:4096\n"
        "\tv_mfma_f32_16x16x32_bf16 a[248:251], v[28:31], v[28:31], 0\n"
        "\tds_read_b64_tr_b16 v[46:47], v4 offset:5120\n"
        "\tv_mfma_f32_16x16x32_bf16 v[152:155], v[208:211], v[24:27], 0\n"
        "\tds_read_b128 v[224:227], v1 offset:7168\n"
        "\ts_nop 0\n"
        "\tv_cmp_u_f32 vcc, v192, v196\n"
        "\tv_mfma_f32_16x16x32_bf16 v[156:159], v[208:211], v[28:31], 0\n"
        "\tv_mfma_f32_16x16x32_bf16 v[160:163], v[212:215], v[24:27], 0\n"
        "\tds_read_b128 v[228:231], v1 offset:8192\n"
        "\ts_cbranch_vccnz LRa1\n"
        "LBa1:\n"
        "\tv_mfma_f32_16x16x32_bf16 v[164:167], v[212:215], v[28:31], 0\n"
        "\ts_add_u32 m0, s67, 4096\n"
        "\tv_mfma_f32_16x16x32_bf16 v[168:171], v[216:219], v[24:27], 0\n"
        "\tds_read_b128 v[232:235], v1 offset:9216\n"
        "\ts_add_u32 s96, s73, 16384\n"
        "\tv_mfma_f32_16x16x32_bf16 v[172:175], v[216:219], v[28:31], 0\n"
        "\ts_waitcnt lgkmcnt(3)\n"
        "\tbuffer_load_dwordx4 v2, s[8:11], s96 offen sc1 lds\n"
        "\tv_mfma_f32_16x16x32_bf16 v[176:179], v[220:223], v[24:27], 0\n"
        "\tds_read_b128 v[236:239], v1 offset:10240\n"
        "\tv_mfma_f32_16x16x32_bf16 v[180:183], v[220:223], v[28:31], 0\n"
        "\tbuffer_load_dwordx4 v2, s[8:11], s96 offen offset:1024 sc1 lds\n"
        "\ts_waitcnt vmcnt(10)\n"
        "\tv_mfma_f32_16x16x32_bf16 v[184:187], a[224:227], v[40:43], v[192:195]\n"
        "\tds_read_b64_tr_b16 v[48:49], v3 offset:6144\n"
        "\tv_mfma_f32_16x16x32_bf16 v[188:191], a[224:227], v[44:47], v[196:199]\n"
        "\tds_read_b64_tr_b16 v[50:51], v3 offset:7168\n"
        "\tv_mfma_f32_16x16x32_bf16 a[244:247], v[32:35], v[32:35], a[244:247]\n"
        "\tds_read_b64_tr_b16 v[52:53], v4 offset:6144\n"
        "\tv_mfma_f32_16x16x32_bf16 a[248:251], v[36:39], v[36:39], a[248:251]\n"
        "\tds_read_b64_tr_b16 v[54:55], v4 offset:7168\n"
        "\ts_waitcnt lgkmcnt(7)\n"
        "\tv_mfma_f32_16x16x32_bf16 v[152:155], v[224:227], v[32:35], v[152:155]\n"
        "\tds_read_b128 v[208:211], v1 offset:14336\n"
        "\tv_cmp_u_f32 vcc, v184, v188\n"
        "\tv_mfma_f32_16x16x32_bf16 v[156:159], v[224:227], v[36:39], v[156:159]\n"
        "\ts_waitcnt lgkmcnt(7)\n"
        "\tv_mfma_f32_16x16x32_bf16 v[160:163], v[228:231], v[32:35], v[160:163]\n"
        "\tds_read_b128 v[212:215], v1 offset:15360\n"
        "\ts_cbranch_vccnz LRa2\n"
        "LBa2:\n"
        "\tv_mfma_f32_16x16x32_bf16 v[164:167], v[228:231], v[36:39], v[164:167]\n"
        "\ts_add_u32 m0, s67, 6144\n"
        "\ts_waitcnt lgkmcnt(7)\n"
        "\tv_mfma_f32_16x16x32_bf16 v[168:171], v[232:235], v[32:35], v[168:171]\n"
        "\tds_read_b128 v[216:219], v1 offset:16384\n"
        "\ts_add_u32 s96, s73, 18432\n"
        "\tv_mfma_f32_16x16x32_bf16 v[172:175], v[232:235], v[36:39], v[172:175]\n"
        "\ts_waitcnt lgkmcnt(3)\n"
        "\tbuffer_load_dwordx4 v2, s[8:11], s96 offen sc1 lds\n"
        "\tv_mfma_f32_16x16x32_bf16 v[176:179], v[236:239], v[32:35], v[176:179]\n"
        "\tds_read_b128 v[220:223], v1 offset:17408\n"
        "\tv_mfma_f32_16x16x32_bf16 v[180:183], v[236:239], v[36:39], v[180:183]\n"
        "\tbuffer_load_dwordx4 v2, s[8:11], s96 offen offset:1024 sc1 lds\n"
        "\ts_waitcnt vmcnt(10)\n"
        "\tv_mfma_f32_16x16x32_bf16 v[192:195], a[224:227], v[48:51], v[184:187]\n"
        "\tds_read_b64_tr_b16 v[56:57], v3 offset:8192\n"
        "\tv_mfma_f32_16x16x32_bf16 v[196:199], a[224:227], v[52:55], v[188:191]\n"
        "\tds_read_b64_tr_b16 v[58:59], v3 offset:9216\n"
        "\tv_mfma_f32_16x16x32_bf16 a[244:247], v[40:43], v[40:43], a[244:247]\n"
        "\tds_read_b64_tr_b16 v[60:61], v4 offset:8192\n"
        "\tv_mfma_f32_16x16x32_bf16 a[248:251], v[44:47], v[44:47], a[248:251]\n"
        "\tds_read_b64_tr_b16 v[62:63], v4 offset:9216\n"
        "\ts_waitcnt lgkmcnt(7)\n"
        "\tv_mfma_f32_16x16x32_bf16 v[152:155], v[208:211], v[40:43], v[152:155]\n"
        "\tds_read_b128 v[224:227], v1 offset:21504\n"
        "\tv_cmp_u_f32 vcc, v192, v196\n"
        "\tv_mfma_f32_16x16x32_bf16 v[156:159], v[208:211], v[44:47], v[156:159]\n"
        "\ts_waitcnt lgkmcnt(7)\n"
        "\tv_mfma_f32_16x16x32_bf16 v[160:163], v[212:215], v[40:43], v[160:163]\n"
        "\tds_read_b128 v[228:231], v1 offset:22528\n"
        "\ts_cbranch_vccnz LRa3\n"
        "LBa3:\n"
        "\tv_mfma_f32_16x16x32_bf16 v[164:167], v[212:215], v[44:47], v[164:167]\n"
        "\ts_add_u32 m0, s67, 8192\n"
        "\ts_waitcnt lgkmcnt(7)\n"
        "\tv_mfma_f32_16x16x32_bf16 v[168:171], v[216:219], v[40:43], v[168:171]\n"
        "\tds_read_b128 v[232:235], v1 offset:23552\n"
        "\ts_add_u32 s96, s73, 20480\n"
        "\tv_mfma_f32_16x16x32_bf16 v[172:175], v[216:219], v[44:47], v[172:175]\n"
        "\ts_waitcnt lgkmcnt(3)\n"
        "\tbuffer_load_dwordx4 v2, s[8:11], s96 offen sc1 lds\n"
        "\tv_mfma_f32_16x16x32_bf16 v[176:179], v[220:223], v[40:43], v[176:179]\n"
        "\tds_read_b128 v[236:239], v1 offset:24576\n"
        "\tv_mfma_f32_16x16x32_bf16 v[180:183], v[220:223], v[44:47], v[180:183]\n"
        "\tbuffer_load_dwordx4 v2, s[8:11], s96 offen offset:1024 sc1 lds\n"
        "\ts_waitcnt vmcnt(10)\n"
        "\tv_mfma_f32_16x16x32_bf16 v[184:187], a[224:227], v[56:59], v[192:195]\n"
        "\tds_read_b64_tr_b16 v[64:65], v3 offset:10240\n"
        "\tv_mfma_f32_16x16x32_bf16 v[188:191], a[224:227], v[60:63], v[196:199]\n"
        "\tds_read_b64_tr_b16 v[66:67], v3 offset:11264\n"
        "\tv_mfma_f32_16x16x32_bf16 a[244:247], v[48:51], v[48:51], a[244:247]\n"
        "\tds_read_b64_tr_b16 v[68:69], v4 offset:10240\n"
        "\tv_mfma_f32_16x16x32_bf16 a[248:251], v[52:55], v[52:55], a[248:251]\n"
        "\tds_read_b64_tr_b16 v[70:71], v4 offset:11264\n"
        "\ts_waitcnt lgkmcnt(7)\n"
        "\tv_mfma_f32_16x16x32_bf16 v[152:155], v[224:227], v[48:51], v[152:155]\n"
        "\tds_read_b128 v[208:211], v1 offset:28672\n"
        "\tv_cmp_u_f32 vcc, v184, v188\n"
        "\tv_mfma_f32_16x16x32_bf16 v[156:159], v[224:227], v[52:55], v[156:159]\n"
        "\ts_waitcnt lgkmcnt(7)\n"
        "\tv_mfma_f32_16x16x32_bf16 v[160:163], v[228:231], v[48:51], v[160:163]\n"
        "\tds_read_b128 v[212:215], v1 offset:29696\n"
        "\ts_cbranch_vccnz LRa4\n"
        "LBa4:\n"
        "\tv_mfma_f32_16x16x32_bf16 v[164:167], v[228:231], v[52:55], v[164:167]\n"
        "\ts_add_u32 m0, s67, 10240\n"
        "\ts_waitcnt lgkmcnt(7)\n"
        "\tv_mfma_f32_16x16x32_bf16 v[168:171], v[232:235], v[48:51], v[168:171]\n"
        "\tds_read_b128 v[216:219], v1 offset:30720\n"
        "\ts_add_u32 s96, s73, 22528\n"
        "\tv_mfma_f32_16x16x32_bf16 v[172:175], v[232:235], v[52:55], v[172:175]\n"
        "\ts_waitcnt lgkmcnt(3)\n"
        "\tbuffer_load_dwordx4 v2, s[8:11], s96 offen sc1 lds\n"
        "\tv_mfma_f32_16x16x32_bf16 v[176:179], v[236:239], v[48:51], v[176:179]\n"
        "\tds_read_b128 v[220:223], v1 offset:31744\n"
        "\tv_mfma_f32_16x16x32_bf16 v[180:183], v[236:239], v[52:55], v[180:183]\n"
        "\tbuffer_load_dwordx4 v2, s[8:11], s96 offen offset:1024 sc1 lds\n"
        "\ts_waitcnt vmcnt(10)\n"
        "\tv_mfma_f32_16x16x32_bf16 v[192:195], a[224:227], v[64:67], v[184:187]\n"
        "\tds_read_b64_tr_b16 v[72:73], v3 offset:0\n"
        "\tv_mfma_f32_16x16x32_bf16 v[196:199], a[224:227], v[68:71], v[188:191]\n"
        "\tds_read_b64_tr_b16 v[74:75], v3 offset:1024\n"
        "\tv_mfma_f32_16x16x32_bf16 a[244:247], v[56:59], v[56:59], a[244:247]\n"
        "\tds_read_b64_tr_b16 v[76:77], v4 offset:0\n"
        "\tv_mfma_f32_16x16x32_bf16 a[248:251], v[60:63], v[60:63], a[248:251]\n"
        "\tds_read_b64_tr_b16 v[78:79], v4 offset:1024\n"
        "\ts_waitcnt lgkmcnt(7)\n"
        "\tv_mfma_f32_16x16x32_bf16 v[152:155], v[208:211], v[56:59], v[152:155]\n"
        "\tds_read_b128 v[224:227], v1 offset:35840\n"
        "\tv_cmp_u_f32 vcc, v192, v196\n"
        "\tv_mfma_f32_16x16x32_bf16 v[156:159], v[208:211], v[60:63], v[156:159]\n"
        "\ts_waitcnt lgkmcnt(7)\n"
        "\tv_mfma_f32_16x16x32_bf16 v[160:163], v[212:215], v[56:59], v[160:163]\n"
        "\tds_read_b128 v[228:231], v1 offset:36864\n"
        "\ts_cbranch_vccnz LRa5\n"
        "LBa5:\n"
        "\tv_mfma_f32_16x16x32_bf16 v[164:167], v[212:215], v[60:63], v[164:167]\n"
        "\ts_add_u32 m0, s67, 0\n"
        "\ts_waitcnt lgkmcnt(7)\n"
        "\tv_mfma_f32_16x16x32_bf16 v[168:171], v[216:219], v[56:59], v[168:171]\n"
        "\tds_read_b128 v[232:235], v1 offset:37888\n"
        "\ts_add_u32 s96, s73, 24576\n"
        "\tv_mfma_f32_16x16x32_bf16 v[172:175], v[216:219], v[60:63], v[172:175]\n"
        "\ts_waitcnt lgkmcnt(3)\n"
        "\tbuffer_load_dwordx4 v2, s[8:11], s96 offen sc1 lds\n"
        "\tv_mfma_f32_16x16x32_bf16 v[176:179], v[220:223], v[56:59], v[176:179]\n"
        "\tds_read_b128 v[236:239], v1 offset:38912\n"
        "\tv_mfma_f32_16x16x32_bf16 v[180:183], v[220:223], v[60:63], v[180:183]\n"
        "\tbuffer_load_dwordx4 v2, s[8:11], s96 offen offset:1024 sc1 lds\n"
        "\ts_waitcnt vmcnt(10)\n"
        "\tv_mfma_f32_16x16x32_bf16 v[184:187], a[224:227], v[72:75], v[192:195]\n"
        "\tds_read_b64_tr_b16 v[80:81], v3 offset:2048\n"
        "\tv_mfma_f32_16x16x32_bf16 v[188:191], a[224:227], v[76:79], v[196:199]\n"
        "\tds_read_b64_tr_b16 v[82:83], v3 offset:3072\n"
        "\tv_mfma_f32_16x16x32_bf16 a[244:247], v[64:67], v[64:67], a[244:247]\n"
        "\tds_read_b64_tr_b16 v[84:85], v4 offset:2048\n"
        "\tv_mfma_f32_16x16x32_bf16 a[248:251], v[68:71], v[68:71], a[248:251]\n"
        "\tds_read_b64_tr_b16 v[86:87], v4 offset:3072\n"
        "\ts_waitcnt lgkmcnt(7)\n"
        "\tv_mfma_f32_16x16x32_bf16 v[152:155], v[224:227], v[64:67], v[152:155]\n"
        "\tds_read_b128 v[208:211], v1 offset:43008\n"
        "\tv_cmp_u_f32 vcc, v184, v188\n"
        "\tv_mfma_f32_16x16x32_bf16 v[156:159], v[224:227], v[68:71], v[156:159]\n"
        "\ts_waitcnt lgkmcnt(7)\n"
        "\tv_mfma_f32_16x16x32_bf16 v[160:163], v[228:231], v[64:67], v[160:163]\n"
        "\tds_read_b128 v[212:215], v1 offset:44032\n"
        "\ts_cbranch_vccnz LRa6\n"
        "LBa6:\n"
        "\tv_mfma_f32_16x16x32_bf16 v[164:167], v[228:231], v[68:71], v[164:167]\n"
        "\ts_add_u32 m0, s67, 2048\n"
        "\ts_waitcnt lgkmcnt(7)\n"
        "\tv_mfma_f32_16x16x32_bf16 v[168:171], v[232:235], v[64:67], v[168:171]\n"
        "\tds_read_b128 v[216:219], v1 offset:45056\n"
        "\ts_add_u32 s96, s73, 26624\n"
        "\tv_mfma_f32_16x16x32_bf16 v[172:175], v[232:235], v[68:71], v[172:175]\n"
        "\ts_waitcnt lgkmcnt(3)\n"
        "\tbuffer_load_dwordx4 v2, s[8:11], s96 offen sc1 lds\n"
        "\tv_mfma_f32_16x16x32_bf16 v[176:179], v[236:239], v[64:67], v[176:179]\n"
        "\tds_read_b128 v[220:223], v1 offset:46080\n"
        "\tv_mfma_f32_16x16x32_bf16 v[180:183], v[236:239], v[68:71], v[180:183]\n"
        "\tbuffer_load_dwordx4 v2, s[8:11], s96 offen offset:1024 sc1 lds\n"
        "\ts_waitcnt vmcnt(10)\n"
        "\tv_mfma_f32_16x16x32_bf16 v[192:195], a[224:227], v[80:83], v[184:187]\n"
        "\tds_read_b64_tr_b16 v[88:89], v3 offset:4096\n"
        "\tv_mfma_f32_16x16x32_bf16 v[196:199], a[224:227], v[84:87], v[188:191]\n"
        "\tds_read_b64_tr_b16 v[90:91], v3 offset:5120\n"
        "\tv_mfma_f32_16x16x32_bf16 a[244:247], v[72:75], v[72:75], a[244:247]\n"
        "\tds_read_b64_tr_b16 v[92:93], v4 offset:4096\n"
        "\tv_mfma_f32_16x16x32_bf16 a[248:251], v[76:79], v[76:79], a[248:251]\n"
        "\tds_read_b64_tr_b16 v[94:95], v4 offset:5120\n"
        "\ts_waitcnt lgkmcnt(7)\n"
        "\tv_mfma_f32_16x16x32_bf16 v[152:155], v[208:211], v[72:75], v[152:155]\n"
        "\tds_read_b128 v[224:227], v1 offset:50176\n"
        "\tv_cmp_u_f32 vcc, v192, v196\n"
        "\tv_mfma_f32_16x16x32_bf16 v[156:159], v[208:211], v[76:79], v[156:159]\n"
        "\ts_waitcnt lgkmcnt(7)\n"
        "\tv_mfma_f32_16x16x32_bf16 v[160:163], v[212:215], v[72:75], v[160:163]\n"
        "\tds_read_b128 v[228:231], v1 offset:51200\n"
        "\ts_cbranch_vccnz LRa7\n"
        "LBa7:\n"
        "\tv_mfma_f32_16x16x32_bf16 v[164:167], v[212:215], v[76:79], v[164:167]\n"
        "\ts_add_u32 m0, s67, 4096\n"
        "\ts_waitcnt lgkmcnt(7)\n"
        "\tv_mfma_f32_16x16x32_bf16 v[168:171], v[216:219], v[72:75], v[168:171]\n"
        "\tds_read_b128 v[232:235], v1 offset:52224\n"
        "\ts_add_u32 s96, s73, 28672\n"
        "\tv_mfma_f32_16x16x32_bf16 v[172:175], v[216:219], v[76:79], v[172:175]\n"
        "\ts_waitcnt lgkmcnt(3)\n"
        "\tbuffer_load_dwordx4 v2, s[8:11], s96 offen sc1 lds\n"
        "\tv_mfma_f32_16x16x32_bf16 v[176:179], v[220:223], v[72:75], v[176:179]\n"
        "\tds_read_b128 v[236:239], v1 offset:53248\n"
        "\tv_mfma_f32_16x16x32_bf16 v[180:183], v[220:223], v[76:79], v[180:183]\n"
        "\tbuffer_load_dwordx4 v2, s[8:11], s96 offen offset:1024 sc1 lds\n"
        "\ts_waitcnt vmcnt(10)\n"
        "\tv_mfma_f32_16x16x32_bf16 v[184:187], a[224:227], v[88:91], v[192:195]\n"
        "\tds_read_b64_tr_b16 v[96:97], v3 offset:6144\n"
        "\tv_mfma_f32_16x16x32_bf16 v[188:191], a[224:227], v[92:95], v[196:199]\n"
        "\tds_read_b64_tr_b16 v[98:99], v3 offset:7168\n"
        "\tv_mfma_f32_16x16x32_bf16 a[244:247], v[80:83], v[80:83], a[244:247]\n"
        "\tds_read_b64_tr_b16 v[100:101], v4 offset:6144\n"
        "\tv_mfma_f32_16x16x32_bf16 a[248:251], v[84:87], v[84:87], a[248:251]\n"
        "\tds_read_b64_tr_b16 v[102:103], v4 offset:7168\n"
        "\tbuffer_load_dwordx2 v[240:241], v242, s[8:11], s33 offen sc1\n"
        "\ts_waitcnt lgkmcnt(7)\n"
        "\tv_mfma_f32_16x16x32_bf16 v[152:155], v[224:227], v[80:83], v[152:155]\n"
        "\tds_read_b128 v[208:211], v6 offset:0\n"
        "\tv_cmp_u_f32 vcc, v184, v188\n"
        "\tv_mfma_f32_16x16x32_bf16 v[156:159], v[224:227], v[84:87], v[156:159]\n"
        "\ts_waitcnt lgkmcnt(7)\n"
        "\tv_mfma_f32_16x16x32_bf16 v[160:163], v[228:231], v[80:83], v[160:163]\n"
        "\tds_read_b128 v[212:215], v6 offset:1024\n"
        "\ts_cbranch_vccnz LRa8\n"
        "LBa8:\n"
        "\tv_mfma_f32_16x16x32_bf16 v[164:167], v[228:231], v[84:87], v[164:167]\n"
        "\ts_add_u32 m0, s67, 6144\n"
        "\ts_waitcnt lgkmcnt(7)\n"
        "\tv_mfma_f32_16x16x32_bf16 v[168:171], v[232:235], v[80:83], v[168:171]\n"
        "\tds_read_b128 v[216:219], v6 offset:2048\n"
        "\ts_add_u32 s96, s73, 30720\n"
        "\tv_mfma_f32_16x16x32_bf16 v[172:175], v[232:235], v[84:87], v[172:175]\n"
        "\ts_waitcnt lgkmcnt(3)\n"
        "\tbuffer_load_dwordx4 v2, s[8:11], s96 offen sc1 lds\n"
        "\tv_mfma_f32_16x16x32_bf16 v[176:179], v[236:239], v[80:83], v[176:179]\n"
        "\tds_read_b128 v[220:223], v6 offset:3072\n"
        "\tv_mfma_f32_16x16x32_bf16 v[180:183], v[236:239], v[84:87], v[180:183]\n"
        "\tbuffer_load_dwordx4 v2, s[8:11], s96 offen offset:1024 sc1 lds\n"
        "\ts_waitcnt vmcnt(11)\n"
        "\tv_mfma_f32_16x16x32_bf16 v[192:195], a[224:227], v[96:99], v[184:187]\n"
        "\tds_read_b64_tr_b16 v[104:105], v3 offset:8192\n"
        "\tv_mfma_f32_16x16x32_bf16 v[196:199], a[224:227], v[100:103], v[188:191]\n"
        "\tds_read_b64_tr_b16 v[106:107], v3 offset:9216\n"
        "\tv_mfma_f32_16x16x32_bf16 a[244:247], v[88:91], v[88:91], a[244:247]\n"
        "\tds_read_b64_tr_b16 v[108:109], v4 offset:8192\n"
        "\tv_mfma_f32_16x16x32_bf16 a[248:251], v[92:95], v[92:95], a[248:251]\n"
        "\tds_read_b64_tr_b16 v[110:111], v4 offset:9216\n"
        "\ts_waitcnt lgkmcnt(7)\n"
        "\tv_mfma_f32_16x16x32_bf16 v[152:155], v[208:211], v[88:91], v[152:155]\n"
        "\tds_read_b128 v[224:227], v6 offset:7168\n"
        "\tv_cmp_u_f32 vcc, v192, v196\n"
        "\tv_mfma_f32_16x16x32_bf16 v[156:159], v[208:211], v[92:95], v[156:159]\n"
        "\ts_waitcnt lgkmcnt(7)\n"
        "\tv_mfma_f32_16x16x32_bf16 v[160:163], v[212:215], v[88:91], v[160:163]\n"
        "\tds_read_b128 v[228:231], v6 offset:8192\n"
        "\ts_cbranch_vccnz LRa9\n"
        "LBa9:\n"
        "\tv_mfma_f32_16x16x32_bf16 v[164:167], v[212:215], v[92:95], v[164:167]\n"
        "\ts_waitcnt lgkmcnt(7)\n"
        "\tv_mfma_f32_16x16x32_bf16 v[168:171], v[216:219], v[88:91], v[168:171]\n"
        "\tds_read_b128 v[232:235], v6 offset:9216\n"
        "\tv_mfma_f32_16x16x32_bf16 v[172:175], v[216:219], v[92:95], v[172:175]\n"
        "\ts_waitcnt lgkmcnt(7)\n"
        "\tv_mfma_f32_16x16x32_bf16 v[176:179], v[220:223], v[88:91], v[176:179]\n"
        "\tds_read_b128 v[236:239], v6 offset:10240\n"
        "\tv_mfma_f32_16x16x32_bf16 v[180:183], v[220:223], v[92:95], v[180:183]\n"
        "\ts_waitcnt vmcnt(9) lgkmcnt(6)\n"
        "\tv_mfma_f32_16x16x32_bf16 v[184:187], a[224:227], v[104:107], v[192:195]\n"
        "\tds_read_b64_tr_b16 v[112:113], v3 offset:10240\n"
        "\ts_waitcnt lgkmcnt(5)\n"
        "\tv_mfma_f32_16x16x32_bf16 v[188:191], a[224:227], v[108:111], v[196:199]\n"
        "\tds_read_b64_tr_b16 v[114:115], v3 offset:11264\n"
        "\tv_mfma_f32_16x16x32_bf16 a[244:247], v[96:99], v[96:99], a[244:247]\n"
        "\tds_read_b64_tr_b16 v[116:117], v4 offset:10240\n"
        "\tv_mfma_f32_16x16x32_bf16 a[248:251], v[100:103], v[100:103], a[248:251]\n"
        "\tds_read_b64_tr_b16 v[118:119], v4 offset:11264\n"
        "\ts_waitcnt lgkmcnt(7)\n"
        "\tv_mfma_f32_16x16x32_bf16 v[152:155], v[224:227], v[96:99], v[152:155]\n"
        "\tds_read_b128 v[208:211], v6 offset:14336\n"
        "\tv_cmp_u_f32 vcc, v184, v188\n"
        "\tv_mfma_f32_16x16x32_bf16 v[156:159], v[224:227], v[100:103], v[156:159]\n"
        "\ts_waitcnt lgkmcnt(7)\n"
        "\tv_mfma_f32_16x16x32_bf16 v[160:163], v[228:231], v[96:99], v[160:163]\n"
        "\tds_read_b128 v[212:215], v6 offset:15360\n"
        "\ts_cbranch_vccnz LRa10\n"
        "LBa10:\n"
        "\tv_mfma_f32_16x16x32_bf16 v[164:167], v[228:231], v[100:103], v[164:167]\n"
        "\ts_waitcnt lgkmcnt(7)\n"
        "\tv_mfma_f32_16x16x32_bf16 v[168:171], v[232:235], v[96:99], v[168:171]\n"
        "\tds_read_b128 v[216:219], v6 offset:16384\n"
        "\tv_mfma_f32_16x16x32_bf16 v[172:175], v[232:235], v[100:103], v[172:175]\n"
        "\ts_waitcnt lgkmcnt(7)\n"
        "\tv_mfma_f32_16x16x32_bf16 v[176:179], v[236:239], v[96:99], v[176:179]\n"
        "\tds_read_b128 v[220:223], v6 offset:17408\n"
        "\tv_mfma_f32_16x16x32_bf16 v[180:183], v[236:239], v[100:103], v[180:183]\n"
        "\ts_waitcnt vmcnt(7) lgkmcnt(6)\n"
        "\tv_mfma_f32_16x16x32_bf16 v[192:195], a[224:227], v[112:115], v[184:187]\n"
        "\tds_read_b64_tr_b16 v[120:121], v3 offset:0\n"
        "\ts_waitcnt lgkmcnt(5)\n"
        "\tv_mfma_f32_16x16x32_bf16 v[196:199], a[224:227], v[116:119], v[188:191]\n"
        "\tds_read_b64_tr_b16 v[122:123], v3 offset:1024\n"
        "\tv_mfma_f32_16x16x32_bf16 a[244:247], v[104:107], v[104:107], a[244:247]\n"
        "\tds_read_b64_tr_b16 v[124:125], v4 offset:0\n"
        "\tv_mfma_f32_16x16x32_bf16 a[248:251], v[108:111], v[108:111], a[248:251]\n"
        "\tds_read_b64_tr_b16 v[126:127], v4 offset:1024\n"
        "\ts_waitcnt lgkmcnt(7)\n"
        "\tv_mfma_f32_16x16x32_bf16 v[152:155], v[208:211], v[104:107], v[152:155]\n"
        "\tds_read_b128 v[224:227], v6 offset:21504\n"
        "\tv_cmp_u_f32 vcc, v192, v196\n"
        "\tv_mfma_f32_16x16x32_bf16 v[156:159], v[208:211], v[108:111], v[156:159]\n"
        "\ts_waitcnt lgkmcnt(7)\n"
        "\tv_mfma_f32_16x16x32_bf16 v[160:163], v[212:215], v[104:107], v[160:163]\n"
        "\tds_read_b128 v[228:231], v6 offset:22528\n"
        "\ts_cbranch_vccnz LRa11\n"
        "LBa11:\n"
        "\tv_mfma_f32_16x16x32_bf16 v[164:167], v[212:215], v[108:111], v[164:167]\n"
        "\ts_add_u32 m0, s67, 8192\n"
        "\ts_waitcnt lgkmcnt(7)\n"
        "\tv_mfma_f32_16x16x32_bf16 v[168:171], v[216:219], v[104:107], v[168:171]\n"
        "\tds_read_b128 v[232:235], v6 offset:23552\n"
        "\tv_mfma_f32_16x16x32_bf16 v[172:175], v[216:219], v[108:111], v[172:175]\n"
        "\tbuffer_load_dwordx4 v1, s[8:11], s81 offen sc1 lds\n"
        "\ts_waitcnt lgkmcnt(7)\n"
        "\tv_mfma_f32_16x16x32_bf16 v[176:179], v[220:223], v[104:107], v[176:179]\n"
        "\tds_read_b128 v[236:239], v6 offset:24576\n"
        "\tv_mfma_f32_16x16x32_bf16 v[180:183], v[220:223], v[108:111], v[180:183]\n"
        "\tbuffer_load_dwordx4 v7, s[8:11], s81 offen offset:1024 sc1 lds\n"
        "\ts_waitcnt vmcnt(7) lgkmcnt(6)\n"
        "\tv_mfma_f32_16x16x32_bf16 v[184:187], a[224:227], v[120:123], v[192:195]\n"
        "\tds_read_b64_tr_b16 v[128:129], v3 offset:2048\n"
        "\ts_waitcnt lgkmcnt(5)\n"
        "\tv_mfma_f32_16x16x32_bf16 v[188:191], a[224:227], v[124:127], v[196:199]\n"
        "\tds_read_b64_tr_b16 v[130:131], v3 offset:3072\n"
        "\tv_mfma_f32_16x16x32_bf16 a[244:247], v[112:115], v[112:115], a[244:247]\n"
        "\tds_read_b64_tr_b16 v[132:133], v4 offset:2048\n"
        "\tv_mfma_f32_16x16x32_bf16 a[248:251], v[116:119], v[116:119], a[248:251]\n"
        "\tds_read_b64_tr_b16 v[134:135], v4 offset:3072\n"
        "\ts_waitcnt lgkmcnt(7)\n"
        "\tv_mfma_f32_16x16x32_bf16 v[152:155], v[224:227], v[112:115], v[152:155]\n"
        "\tds_read_b128 v[208:211], v6 offset:28672\n"
        "\tv_cmp_u_f32 vcc, v184, v188\n"
        "\tv_mfma_f32_16x16x32_bf16 v[156:159], v[224:227], v[116:119], v[156:159]\n"
        "\ts_waitcnt lgkmcnt(7)\n"
        "\tv_mfma_f32_16x16x32_bf16 v[160:163], v[228:231], v[112:115], v[160:163]\n"
        "\tds_read_b128 v[212:215], v6 offset:29696\n"
        "\ts_cbranch_vccnz LRa12\n"
        "LBa12:\n"
        "\tv_mfma_f32_16x16x32_bf16 v[164:167], v[228:231], v[116:119], v[164:167]\n"
        "\ts_waitcnt lgkmcnt(7)\n"
        "\tv_mfma_f32_16x16x32_bf16 v[168:171], v[232:235], v[112:115], v[168:171]\n"
        "\tds_read_b128 v[216:219], v6 offset:30720\n"
        "\tv_mfma_f32_16x16x32_bf16 v[172:175], v[232:235], v[116:119], v[172:175]\n"
        "\ts_waitcnt lgkmcnt(7)\n"
        "\tv_mfma_f32_16x16x32_bf16 v[176:179], v[236:239], v[112:115], v[176:179]\n"
        "\tds_read_b128 v[220:223], v6 offset:31744\n"
        "\tv_mfma_f32_16x16x32_bf16 v[180:183], v[236:239], v[116:119], v[180:183]\n"
        "\ts_waitcnt vmcnt(5) lgkmcnt(6)\n"
        "\tv_mfma_f32_16x16x32_bf16 v[192:195], a[224:227], v[128:131], v[184:187]\n"
        "\tds_read_b64_tr_b16 v[136:137], v3 offset:4096\n"
        "\ts_waitcnt vmcnt(4)\n"
        "\tv_xor_b32 v242, v240, v20\n"
        "\ts_waitcnt lgkmcnt(5)\n"
        "\tv_mfma_f32_16x16x32_bf16 v[196:199], a[224:227], v[132:135], v[188:191]\n"
        "\tds_read_b64_tr_b16 v[138:139], v3 offset:5120\n"
        "\tv_xor_b32 v243, v241, v21\n"
        "\tv_mfma_f32_16x16x32_bf16 a[244:247], v[120:123], v[120:123], a[244:247]\n"
        "\tds_read_b64_tr_b16 v[140:141], v4 offset:4096\n"
        "\tv_and_or_b32 v242, v242, s83, v243\n"
        "\tv_mfma_f32_16x16x32_bf16 a[248:251], v[124:127], v[124:127], a[248:251]\n"
        "\tds_read_b64_tr_b16 v[142:143], v4 offset:5120\n"
        "\tv_subrev_u32 v240, s32, v240\n"
        "\ts_waitcnt lgkmcnt(7)\n"
        "\tv_mfma_f32_16x16x32_bf16 v[152:155], v[208:211], v[120:123], v[152:155]\n"
        "\tds_read_b128 v[224:227], v6 offset:35840\n"
        "\tv_cmp_u_f32 vcc, v192, v196\n"
        "\tv_mfma_f32_16x16x32_bf16 v[156:159], v[208:211], v[124:127], v[156:159]\n"
        "\tv_cmp_eq_u32 s[58:59], 0, v242\n"
        "\ts_waitcnt lgkmcnt(7)\n"
        "\tv_mfma_f32_16x16x32_bf16 v[160:163], v[212:215], v[120:123], v[160:163]\n"
        "\tds_read_b128 v[228:231], v6 offset:36864\n"
        "\ts_cbranch_vccnz LRa13\n"
        "LBa13:\n"
        "\tv_mfma_f32_16x16x32_bf16 v[164:167], v[212:215], v[124:127], v[164:167]\n"
        "\ts_waitcnt lgkmcnt(7)\n"
        "\tv_mfma_f32_16x16x32_bf16 v[168:171], v[216:219], v[120:123], v[168:171]\n"
        "\tds_read_b128 v[232:235], v6 offset:37888\n"
        "\tv_mfma_f32_16x16x32_bf16 v[172:175], v[216:219], v[124:127], v[172:175]\n"
        "\ts_waitcnt lgkmcnt(7)\n"
        "\tv_mfma_f32_16x16x32_bf16 v[176:179], v[220:223], v[120:123], v[176:179]\n"
        "\tds_read_b128 v[236:239], v6 offset:38912\n"
        "\tv_bfe_i32 v240, v240, 0, 16\n"
        "\tv_mfma_f32_16x16x32_bf16 v[180:183], v[220:223], v[124:127], v[180:183]\n"
        "\ts_waitcnt vmcnt(2) lgkmcnt(6)\n"
        "\tv_mfma_f32_16x16x32_bf16 v[184:187], a[224:227], v[136:139], v[192:195]\n"
        "\tds_read_b64_tr_b16 v[144:145], v3 offset:6144\n"
        "\ts_waitcnt vmcnt(0)\n"
        "\ts_barrier\n"
        "\ts_waitcnt lgkmcnt(5)\n"
        "\tv_mfma_f32_16x16x32_bf16 v[188:191], a[224:227], v[140:143], v[196:199]\n"
        "\tds_read_b64_tr_b16 v[146:147], v3 offset:7168\n"
        "\tv_max_i32 v240, 0, v240\n"
        "\tv_mfma_f32_16x16x32_bf16 a[244:247], v[128:131], v[128:131], a[244:247]\n"
        "\tds_read_b64_tr_b16 v[148:149], v4 offset:6144\n"
        "\tv_cndmask_b32 v240, 0, v240, s[58:59]\n"
        "\tv_mfma_f32_16x16x32_bf16 a[248:251], v[132:135], v[132:135], a[248:251]\n"
        "\tds_read_b64_tr_b16 v[150:151], v4 offset:7168\n"
        "\tv_max_i32_dpp v240, v240, v240 row_shr:1 row_mask:0xf bank_mask:0xf\n"
        "\ts_waitcnt lgkmcnt(7)\n"
        "\tv_mfma_f32_16x16x32_bf16 v[152:155], v[224:227], v[128:131], v[152:155]\n"
        "\tds_read_b128 v[208:211], v6 offset:43008\n"
        "\tv_cmp_u_f32 vcc, v184, v188\n"
        "\tv_mfma_f32_16x16x32_bf16 v[156:159], v[224:227], v[132:135], v[156:159]\n"
        "\tds_read_b128 a[192:195], v8 offset:0\n"
        "\ts_waitcnt lgkmcnt(8)\n"
        "\tv_mfma_f32_16x16x32_bf16 v[160:163], v[228:231], v[128:131], v[160:163]\n"
        "\tds_read_b128 v[212:215], v6 offset:44032\n"
        "\ts_cbranch_vccnz LRa14\n"
        "LBa14:\n"
        "\tv_mfma_f32_16x16x32_bf16 v[164:167], v[228:231], v[132:135], v[164:167]\n"
        "\ts_add_u32 m0, s67, 0\n"
        "\tds_read_b128 a[196:199], v8 offset:1024\n"
        "\ts_waitcnt lgkmcnt(9)\n"
        "\tv_mfma_f32_16x16x32_bf16 v[168:171], v[232:235], v[128:131], v[168:171]\n"
        "\tds_read_b128 v[216:219], v6 offset:45056\n"
        "\ts_add_u32 s96, s38, 65536\n"
        "\tv_mfma_f32_16x16x32_bf16 v[172:175], v[232:235], v[132:135], v[172:175]\n"
        "\tbuffer_load_dwordx4 v244, s[12:15], s96 offen offset:0 lds\n"
        "\tds_read_b128 a[200:203], v8 offset:12288\n"
        "\ts_waitcnt lgkmcnt(10)\n"
        "\tv_mfma_f32_16x16x32_bf16 v[176:179], v[236:239], v[128:131], v[176:179]\n"
        "\tds_read_b128 v[220:223], v6 offset:46080\n"
        "\tv_max_i32_dpp v240, v240, v240 row_shr:2 row_mask:0xf bank_mask:0xf\n"
        "\tv_mfma_f32_16x16x32_bf16 v[180:183], v[236:239], v[132:135], v[180:183]\n"
        "\tbuffer_load_dwordx4 v245, s[12:15], s96 offen offset:1024 lds\n"
        "\tds_read_b128 a[204:207], v8 offset:13312\n"
        "\ts_waitcnt lgkmcnt(10)\n"
        "\tv_mfma_f32_16x16x32_bf16 v[192:195], a[224:227], v[144:147], v[184:187]\n"
        "\tv_max_i32_dpp v240, v240, v240 row_shr:4 row_mask:0xf bank_mask:0xf\n"
        "\ts_waitcnt lgkmcnt(8)\n"
        "\tv_mfma_f32_16x16x32_bf16 v[196:199], a[224:227], v[148:151], v[188:191]\n"
        "\tbuffer_load_dwordx4 v246, s[12:15], s96 offen offset:2048 lds\n"
        "\tv_max_i32_dpp v240, v240, v240 row_shr:8 row_mask:0xf bank_mask:0xf\n"
        "\tv_mfma_f32_16x16x32_bf16 a[244:247], v[136:139], v[136:139], a[244:247]\n"
        "\tv_readlane_b32 s31, v240, 15\n"
        "\tv_mfma_f32_16x16x32_bf16 a[248:251], v[140:143], v[140:143], a[248:251]\n"
        "\ts_add_u32 s32, s32, 290\n"
        "\ts_waitcnt lgkmcnt(7)\n"
        "\tv_mfma_f32_16x16x32_bf16 v[152:155], v[208:211], v[136:139], v[152:155]\n"
        "\tds_read_b128 v[224:227], v6 offset:50176\n"
        "\tv_cmp_u_f32 vcc, v192, v196\n"
        "\tv_mfma_f32_16x16x32_bf16 v[156:159], v[208:211], v[140:143], v[156:159]\n"
        "\tbuffer_load_dwordx4 v247, s[12:15], s96 offen offset:3072 lds\n"
        "\tds_read_b128 a[208:211], v8 offset:24576\n"
        "\ts_waitcnt lgkmcnt(7)\n"
        "\tv_mfma_f32_16x16x32_bf16 v[160:163], v[212:215], v[136:139], v[160:163]\n"
        "\tds_read_b128 v[228:231], v6 offset:51200\n"
        "\ts_cbranch_vccnz LRa15\n"
        "LBa15:\n"
        "\tv_mfma_f32_16x16x32_bf16 v[164:167], v[212:215], v[140:143], v[164:167]\n"
        "\tds_read_b128 a[212:215], v8 offset:25600\n"
        "\ts_waitcnt lgkmcnt(7)\n"
        "\tv_mfma_f32_16x16x32_bf16 v[168:171], v[216:219], v[136:139], v[168:171]\n"
        "\tds_read_b128 v[232:235], v6 offset:52224\n"
        "\tv_mfma_f32_16x16x32_bf16 v[172:175], v[216:219], v[140:143], v[172:175]\n"
        "\tds_read_b128 a[216:219], v8 offset:36864\n"
        "\ts_waitcnt lgkmcnt(7)\n"
        "\tv_mfma_f32_16x16x32_bf16 v[176:179], v[220:223], v[136:139], v[176:179]\n"
        "\tds_read_b128 v[236:239], v6 offset:53248\n"
        "\ts_add_u32 s31, s31, s32\n"
        "\tv_mfma_f32_16x16x32_bf16 v[180:183], v[220:223], v[140:143], v[180:183]\n"
        "\ts_waitcnt lgkmcnt(6)\n"
        "\tv_mfma_f32_16x16x32_bf16 v[152:155], v[224:227], v[144:147], v[152:155]\n"
        "\tds_read_b128 v[208:211], v1 offset:4096\n"
        "\tv_mul_f32 v20, 0xbb000000, v192\n"
        "\tv_mfma_f32_16x16x32_bf16 v[156:159], v[224:227], v[148:151], v[156:159]\n"
        "\tds_read_b128 v[212:215], v1 offset:5120\n"
        "\tv_mul_f32 v21, 0xbb000000, v196\n"
        "\ts_waitcnt lgkmcnt(6)\n"
        "\tv_mfma_f32_16x16x32_bf16 v[160:163], v[228:231], v[144:147], v[160:163]\n"
        "\tds_read_b128 v[216:219], v1 offset:6144\n"
        "\tv_cvt_pk_bf16_f32 v22, v20, v20\n"
        "\tv_mfma_f32_16x16x32_bf16 v[164:167], v[228:231], v[148:151], v[164:167]\n"
        "\tv_cvt_pk_bf16_f32 v23, v21, v21\n"
        "\tv_accvgpr_write_b32 a228, v22\n"
        "\ts_waitcnt lgkmcnt(5)\n"
        "\tv_mfma_f32_16x16x32_bf16 v[168:171], v[232:235], v[144:147], v[168:171]\n"
        "\tv_accvgpr_write_b32 a229, v22\n"
        "\tv_accvgpr_write_b32 a230, v22\n"
        "\tv_mfma_f32_16x16x32_bf16 v[172:175], v[232:235], v[148:151], v[172:175]\n"
        "\tv_accvgpr_write_b32 a231, v22\n"
        "\tv_accvgpr_write_b32 a232, v23\n"
        "\ts_waitcnt lgkmcnt(3)\n"
        "\tv_mfma_f32_16x16x32_bf16 v[176:179], v[236:239], v[144:147], v[176:179]\n"
        "\tv_accvgpr_write_b32 a233, v23\n"
        "\tv_accvgpr_write_b32 a234, v23\n"
        "\tv_mfma_f32_16x16x32_bf16 v[180:183], v[236:239], v[148:151], v[180:183]\n"
        "\tv_accvgpr_write_b32 a235, v23\n"
        "\ts_add_u32 m0, s67, 4096\n"
        "\tv_mfma_f32_16x16x32_bf16 a[244:247], v[144:147], v[144:147], a[244:247]\n"
        "\ts_add_u32 s96, s39, 65536\n"
        "\tv_mfma_f32_16x16x32_bf16 a[248:251], v[148:151], v[148:151], a[248:251]\n"
        "\tbuffer_load_dwordx4 v244, s[12:15], s96 offen offset:0 lds\n"
        "\tv_mfma_f32_16x16x32_bf16 v[152:155], a[192:195], a[228:231], v[152:155]\n"
        "\tds_read_b128 v[220:223], v1 offset:11264\n"
        "\tv_mfma_f32_16x16x32_bf16 v[156:159], a[192:195], a[232:235], v[156:159]\n"
        "\tds_read_b128 v[224:227], v1 offset:12288\n"
        "\tv_mfma_f32_16x16x32_bf16 v[160:163], a[196:199], a[228:231], v[160:163]\n"
        "\tds_read_b128 v[228:231], v1 offset:13312\n"
        "\tv_mfma_f32_16x16x32_bf16 v[164:167], a[196:199], a[232:235], v[164:167]\n"
        "\tbuffer_load_dwordx4 v245, s[12:15], s96 offen offset:1024 lds\n"
        "\tv_mfma_f32_16x16x32_bf16 v[168:171], a[200:203], a[228:231], v[168:171]\n"
        "\tbuffer_load_dwordx4 v246, s[12:15], s96 offen offset:2048 lds\n"
        "\tv_mfma_f32_16x16x32_bf16 v[172:175], a[200:203], a[232:235], v[172:175]\n"
        "\tbuffer_load_dwordx4 v247, s[12:15], s96 offen offset:3072 lds\n"
        "\tv_mfma_f32_16x16x32_bf16 v[176:179], a[204:207], a[228:231], v[176:179]\n"
        "\tv_mfma_f32_16x16x32_bf16 v[180:183], a[204:207], a[232:235], v[180:183]\n"
        "\ts_waitcnt lgkmcnt(3)\n"
        "\tv_mfma_f32_16x16x32_bf16 v[184:187], v[208:211], v[24:27], 0\n"
        "\tds_read_b128 v[232:235], v1 offset:18432\n"
        "\tv_cvt_pk_bf16_f32 v152, v152, v153\n"
        "\tv_cvt_pk_bf16_f32 v153, v154, v155\n"
        "\tv_pk_max_i16 v152, v152, s29\n"
        "\tv_mfma_f32_16x16x32_bf16 v[188:191], v[208:211], v[28:31], 0\n"
        "\tds_read_b128 v[236:239], v1 offset:19456\n"
        "\tv_pk_max_i16 v153, v153, s29\n"
        "\tv_pk_add_u16 v152, v152, v253\n"
        "\tv_pk_add_u16 v153, v153, v253\n"
        "\tv_mfma_f32_16x16x32_bf16 v[192:195], v[212:215], v[24:27], 0\n"
        "\tds_read_b128 v[240:243], v1 offset:20480\n"
        "\tv_cvt_pk_bf16_f32 v154, v160, v161\n"
        "\tv_cvt_pk_bf16_f32 v155, v162, v163\n"
        "\tv_pk_max_i16 v154, v154, s29\n"
        "\tv_mfma_f32_16x16x32_bf16 v[196:199], v[212:215], v[28:31], 0\n"
        "\tv_pk_max_i16 v155, v155, s29\n"
        "\tv_pk_add_u16 v154, v154, v253\n"
        "\tv_pk_add_u16 v155, v155, v253\n"
        "\tv_mfma_f32_16x16x32_bf16 v[200:203], v[216:219], v[24:27], 0\n"
        "\tbuffer_store_dwordx4 v[152:155], v1, s[8:11], s70 offen offset:0 sc1\n"
        "\tv_mfma_f32_16x16x32_bf16 v[204:207], v[216:219], v[28:31], 0\n"
        "\tv_cvt_pk_bf16_f32 v156, v156, v157\n"
        "\tv_cvt_pk_bf16_f32 v157, v158, v159\n"
        "\tv_pk_max_i16 v156, v156, s29\n"
        "\ts_waitcnt lgkmcnt(3)\n"
        "\tv_mfma_f32_16x16x32_bf16 v[184:187], v[220:223], v[32:35], v[184:187]\n"
        "\tds_read_b128 v[208:211], v1 offset:25600\n"
        "\tv_pk_max_i16 v157, v157, s29\n"
        "\tv_pk_add_u16 v156, v156, v253\n"
        "\tv_pk_add_u16 v157, v157, v253\n"
        "\tv_mfma_f32_16x16x32_bf16 v[188:191], v[220:223], v[36:39], v[188:191]\n"
        "\tds_read_b128 v[212:215], v1 offset:26624\n"
        "\tv_cvt_pk_bf16_f32 v158, v164, v165\n"
        "\tv_cvt_pk_bf16_f32 v159, v166, v167\n"
        "\tv_pk_max_i16 v158, v158, s29\n"
        "\tv_mfma_f32_16x16x32_bf16 v[192:195], v[224:227], v[32:35], v[192:195]\n"
        "\tds_read_b128 v[216:219], v1 offset:27648\n"
        "\tv_pk_max_i16 v159, v159, s29\n"
        "\tv_pk_add_u16 v158, v158, v253\n"
        "\tv_pk_add_u16 v159, v159, v253\n"
        "\tv_mfma_f32_16x16x32_bf16 v[196:199], v[224:227], v[36:39], v[196:199]\n"
        "\tbuffer_store_dwordx4 v[156:159], v1, s[8:11], s71 offen offset:0 sc1\n"
        "\tv_mfma_f32_16x16x32_bf16 v[200:203], v[228:231], v[32:35], v[200:203]\n"
        "\tv_cvt_pk_bf16_f32 v168, v168, v169\n"
        "\tv_mfma_f32_16x16x32_bf16 v[204:207], v[228:231], v[36:39], v[204:207]\n"
        "\tv_cvt_pk_bf16_f32 v169, v170, v171\n"
        "\ts_waitcnt lgkmcnt(3)\n"
        "\tv_mfma_f32_16x16x32_bf16 v[184:187], v[232:235], v[40:43], v[184:187]\n"
        "\tds_read_b128 v[220:223], v1 offset:32768\n"
        "\tv_pk_max_i16 v168, v168, s29\n"
        "\tv_mfma_f32_16x16x32_bf16 v[188:191], v[232:235], v[44:47], v[188:191]\n"
        "\tds_read_b128 v[224:227], v1 offset:33792\n"
        "\tv_pk_max_i16 v169, v169, s29\n"
        "\tv_mfma_f32_16x16x32_bf16 v[192:195], v[236:239], v[40:43], v[192:195]\n"
        "\tds_read_b128 v[228:231], v1 offset:34816\n"
        "\tv_pk_add_u16 v168, v168, v253\n"
        "\tv_mfma_f32_16x16x32_bf16 v[196:199], v[236:239], v[44:47], v[196:199]\n"
        "\tv_pk_add_u16 v169, v169, v253\n"
        "\tv_mfma_f32_16x16x32_bf16 v[200:203], v[240:243], v[40:43], v[200:203]\n"
        "\tv_cvt_pk_bf16_f32 v170, v176, v177\n"
        "\tv_mfma_f32_16x16x32_bf16 v[204:207], v[240:243], v[44:47], v[204:207]\n"
        "\tv_cvt_pk_bf16_f32 v171, v178, v179\n"
        "\ts_waitcnt lgkmcnt(3)\n"
        "\tv_mfma_f32_16x16x32_bf16 v[184:187], v[208:211], v[48:51], v[184:187]\n"
        "\tds_read_b128 v[232:235], v1 offset:39936\n"
        "\tv_pk_max_i16 v170, v170, s29\n"
        "\tv_mfma_f32_16x16x32_bf16 v[188:191], v[208:211], v[52:55], v[188:191]\n"
        "\tds_read_b128 v[236:239], v1 offset:40960\n"
        "\tv_pk_max_i16 v171, v171, s29\n"
        "\tv_mfma_f32_16x16x32_bf16 v[192:195], v[212:215], v[48:51], v[192:195]\n"
        "\tds_read_b128 v[240:243], v1 offset:41984\n"
        "\tv_pk_add_u16 v170, v170, v253\n"
        "\tv_mfma_f32_16x16x32_bf16 v[196:199], v[212:215], v[52:55], v[196:199]\n"
        "\tv_pk_add_u16 v171, v171, v253\n"
        "\tv_mfma_f32_16x16x32_bf16 v[200:203], v[216:219], v[48:51], v[200:203]\n"
        "\tbuffer_store_dwordx4 v[168:171], v1, s[8:11], s70 offen offset:1024 sc1\n"
        "\tv_mfma_f32_16x16x32_bf16 v[204:207], v[216:219], v[52:55], v[204:207]\n"
        "\tv_cvt_pk_bf16_f32 v172, v172, v173\n"
        "\ts_waitcnt lgkmcnt(3)\n"
        "\tv_mfma_f32_16x16x32_bf16 v[184:187], v[220:223], v[56:59], v[184:187]\n"
        "\tds_read_b128 v[208:211], v1 offset:47104\n"
        "\tv_cvt_pk_bf16_f32 v173, v174, v175\n"
        "\tv_mfma_f32_16x16x32_bf16 v[188:191], v[220:223], v[60:63], v[188:191]\n"
        "\tds_read_b128 v[212:215], v1 offset:48128\n"
        "\tv_pk_max_i16 v172, v172, s29\n"
        "\tv_mfma_f32_16x16x32_bf16 v[192:195], v[224:227], v[56:59], v[192:195]\n"
        "\tds_read_b128 v[216:219], v1 offset:49152\n"
        "\tv_pk_max_i16 v173, v173, s29\n"
        "\tv_mfma_f32_16x16x32_bf16 v[196:199], v[224:227], v[60:63], v[196:199]\n"
        "\tv_pk_add_u16 v172, v172, v253\n"
        "\tv_mfma_f32_16x16x32_bf16 v[200:203], v[228:231], v[56:59], v[200:203]\n"
        "\tv_pk_add_u16 v173, v173, v253\n"
        "\tv_mfma_f32_16x16x32_bf16 v[204:207], v[228:231], v[60:63], v[204:207]\n"
        "\tv_cvt_pk_bf16_f32 v174, v180, v181\n"
        "\ts_waitcnt lgkmcnt(3)\n"
        "\tv_mfma_f32_16x16x32_bf16 v[184:187], v[232:235], v[64:67], v[184:187]\n"
        "\tds_read_b128 v[220:223], v1 offset:54272\n"
        "\tv_cvt_pk_bf16_f32 v175, v182, v183\n"
        "\tv_mfma_f32_16x16x32_bf16 v[188:191], v[232:235], v[68:71], v[188:191]\n"
        "\tds_read_b128 v[224:227], v1 offset:55296\n"
        "\tv_pk_max_i16 v174, v174, s29\n"
        "\tv_mfma_f32_16x16x32_bf16 v[192:195], v[236:239], v[64:67], v[192:195]\n"
        "\tds_read_b128 v[228:231], v1 offset:56320\n"
        "\tv_pk_max_i16 v175, v175, s29\n"
        "\tv_mfma_f32_16x16x32_bf16 v[196:199], v[236:239], v[68:71], v[196:199]\n"
        "\tv_pk_add_u16 v174, v174, v253\n"
        "\tv_mfma_f32_16x16x32_bf16 v[200:203], v[240:243], v[64:67], v[200:203]\n"
        "\tv_pk_add_u16 v175, v175, v253\n"
        "\tv_mfma_f32_16x16x32_bf16 v[204:207], v[240:243], v[68:71], v[204:207]\n"
        "\tbuffer_store_dwordx4 v[172:175], v1, s[8:11], s71 offen offset:1024 sc1\n"
        "\ts_waitcnt lgkmcnt(3)\n"
        "\tv_mfma_f32_16x16x32_bf16 v[184:187], v[208:211], v[72:75], v[184:187]\n"
        "\tds_read_b128 v[232:235], v6 offset:4096\n"
        "\ts_waitcnt vmcnt(8)\n"
        "\tds_read_b64_tr_b16 a[160:161], v248 offset:0\n"
        "\tv_mfma_f32_16x16x32_bf16 v[188:191], v[208:211], v[76:79], v[188:191]\n"
        "\tds_read_b128 v[236:239], v6 offset:5120\n"
        "\tds_read_b64_tr_b16 a[162:163], v248 offset:2048\n"
        "\tv_mfma_f32_16x16x32_bf16 v[192:195], v[212:215], v[72:75], v[192:195]\n"
        "\tds_read_b128 v[240:243], v6 offset:6144\n"
        "\tds_read_b64_tr_b16 a[164:165], v249 offset:0\n"
        "\tv_mfma_f32_16x16x32_bf16 v[196:199], v[212:215], v[76:79], v[196:199]\n"
        "\tds_read_b64_tr_b16 a[166:167], v249 offset:2048\n"
        "\tv_mfma_f32_16x16x32_bf16 v[200:203], v[216:219], v[72:75], v[200:203]\n"
        "\tds_read_b64_tr_b16 a[168:169], v250 offset:0\n"
        "\tv_mfma_f32_16x16x32_bf16 v[204:207], v[216:219], v[76:79], v[204:207]\n"
        "\tds_read_b64_tr_b16 a[170:171], v250 offset:2048\n"
        "\ts_waitcnt lgkmcnt(9)\n"
        "\tv_mfma_f32_16x16x32_bf16 v[184:187], v[220:223], v[80:83], v[184:187]\n"
        "\tds_read_b128 v[208:211], v6 offset:11264\n"
        "\tds_read_b64_tr_b16 a[172:173], v251 offset:0\n"
        "\tv_mfma_f32_16x16x32_bf16 v[188:191], v[220:223], v[84:87], v[188:191]\n"
        "\tds_read_b128 v[212:215], v6 offset:12288\n"
        "\tds_read_b64_tr_b16 a[174:175], v251 offset:2048\n"
        "\tv_mfma_f32_16x16x32_bf16 v[192:195], v[224:227], v[80:83], v[192:195]\n"
        "\tds_read_b128 v[216:219], v6 offset:13312\n"
        "\ts_waitcnt vmcnt(4)\n"
        "\tds_read_b64_tr_b16 a[176:177], v248 offset:4096\n"
        "\tv_mfma_f32_16x16x32_bf16 v[196:199], v[224:227], v[84:87], v[196:199]\n"
        "\tds_read_b64_tr_b16 a[178:179], v248 offset:6144\n"
        "\tv_mfma_f32_16x16x32_bf16 v[200:203], v[228:231], v[80:83], v[200:203]\n"
        "\tds_read_b64_tr_b16 a[180:181], v249 offset:4096\n"
        "\tv_mfma_f32_16x16x32_bf16 v[204:207], v[228:231], v[84:87], v[204:207]\n"
        "\tds_read_b64_tr_b16 a[182:183], v249 offset:6144\n"
        "\ts_waitcnt lgkmcnt(13)\n"
        "\tv_mfma_f32_16x16x32_bf16 v[184:187], v[232:235], v[88:91], v[184:187]\n"
        "\tds_read_b128 v[220:223], v6 offset:18432\n"
        "\tds_read_b64_tr_b16 a[184:185], v250 offset:4096\n"
        "\tv_mfma_f32_16x16x32_bf16 v[188:191], v[232:235], v[92:95], v[188:191]\n"
        "\tds_read_b128 v[224:227], v6 offset:19456\n"
        "\tds_read_b64_tr_b16 a[186:187], v250 offset:6144\n"
        "\tv_mfma_f32_16x16x32_bf16 v[192:195], v[236:239], v[88:91], v[192:195]\n"
        "\tds_read_b128 v[228:231], v6 offset:20480\n"
        "\tds_read_b64_tr_b16 a[188:189], v251 offset:4096\n"
        "\tv_mfma_f32_16x16x32_bf16 v[196:199], v[236:239], v[92:95], v[196:199]\n"
        "\tds_read_b64_tr_b16 a[190:191], v251 offset:6144\n"
        "\tv_mfma_f32_16x16x32_bf16 v[200:203], v[240:243], v[88:91], v[200:203]\n"
        "\ts_waitcnt lgkmcnt(12)\n"
        "\ts_add_u32 m0, s67, 0\n"
        "\tv_mfma_f32_16x16x32_bf16 v[204:207], v[240:243], v[92:95], v[204:207]\n"
        "\ts_add_u32 s96, s64, 98304\n"
        "\ts_waitcnt lgkmcnt(11)\n"
        "\tv_mfma_f32_16x16x32_bf16 v[184:187], v[208:211], v[96:99], v[184:187]\n"
        "\tds_read_b128 v[232:235], v6 offset:25600\n"
        "\tbuffer_load_dwordx4 v244, s[12:15], s96 offen offset:0 lds\n"
        "\tv_mfma_f32_16x16x32_bf16 v[188:191], v[208:211], v[100:103], v[188:191]\n"
        "\tds_read_b128 v[236:239], v6 offset:26624\n"
        "\ts_mov_b32 s58, 0xaaaaaaaa\n"
        "\tv_mfma_f32_16x16x32_bf16 v[192:195], v[212:215], v[96:99], v[192:195]\n"
        "\tds_read_b128 v[240:243], v6 offset:27648\n"
        "\ts_mov_b32 s59, 0xaaaaaaaa\n"
        "\tv_mfma_f32_16x16x32_bf16 v[196:199], v[212:215], v[100:103], v[196:199]\n"
        "\ts_mov_b32 s60, 0xcccccccc\n"
        "\tv_mfma_f32_16x16x32_bf16 v[200:203], v[216:219], v[96:99], v[200:203]\n"
        "\tbuffer_load_dwordx4 v245, s[12:15], s96 offen offset:1024 lds\n"
        "\tv_mfma_f32_16x16x32_bf16 v[204:207], v[216:219], v[100:103], v[204:207]\n"
        "\ts_add_u32 s96, s96, 98304\n"
        "\ts_waitcnt lgkmcnt(5)\n"
        "\tv_mfma_f32_16x16x32_bf16 v[184:187], v[220:223], v[104:107], v[184:187]\n"
        "\tds_read_b128 v[208:211], v6 offset:32768\n"
        "\ts_mov_b32 s61, 0xcccccccc\n"
        "\tv_mfma_f32_16x16x32_bf16 v[188:191], v[220:223], v[108:111], v[188:191]\n"
        "\tds_read_b128 v[212:215], v6 offset:33792\n"
        "\tv_accvgpr_read_b32 v160, a244\n"
        "\tv_mfma_f32_16x16x32_bf16 v[192:195], v[224:227], v[104:107], v[192:195]\n"
        "\tds_read_b128 v[216:219], v6 offset:34816\n"
        "\tbuffer_load_dwordx4 v246, s[12:15], s96 offen offset:2048 lds\n"
        "\tv_mfma_f32_16x16x32_bf16 v[196:199], v[224:227], v[108:111], v[196:199]\n"
        "\tv_accvgpr_read_b32 v161, a245\n"
        "\tv_mfma_f32_16x16x32_bf16 v[200:203], v[228:231], v[104:107], v[200:203]\n"
        "\tv_accvgpr_read_b32 v162, a246\n"
        "\tv_mfma_f32_16x16x32_bf16 v[204:207], v[228:231], v[108:111], v[204:207]\n"
        "\tv_accvgpr_read_b32 v163, a247\n"
        "\ts_waitcnt lgkmcnt(3)\n"
        "\tv_mfma_f32_16x16x32_bf16 v[184:187], v[232:235], v[112:115], v[184:187]\n"
        "\tds_read_b128 v[220:223], v6 offset:39936\n"
        "\tbuffer_load_dwordx4 v247, s[12:15], s96 offen offset:3072 lds\n"
        "\tv_mfma_f32_16x16x32_bf16 v[188:191], v[232:235], v[116:119], v[188:191]\n"
        "\tds_read_b128 v[224:227], v6 offset:40960\n"
        "\ts_add_u32 m0, s67, 4096\n"
        "\tv_mfma_f32_16x16x32_bf16 v[192:195], v[236:239], v[112:115], v[192:195]\n"
        "\tds_read_b128 v[228:231], v6 offset:41984\n"
        "\ts_add_u32 s96, s64, 327680\n"
        "\tv_mfma_f32_16x16x32_bf16 v[196:199], v[236:239], v[116:119], v[196:199]\n"
        "\tv_cndmask_b32 v160, v160, v161, s[58:59]\n"
        "\tv_mfma_f32_16x16x32_bf16 v[200:203], v[240:243], v[112:115], v[200:203]\n"
        "\tbuffer_load_dwordx4 v244, s[12:15], s96 offen offset:0 lds\n"
        "\tv_mfma_f32_16x16x32_bf16 v[204:207], v[240:243], v[116:119], v[204:207]\n"
        "\tv_cndmask_b32 v162, v162, v163, s[58:59]\n"
        "\ts_waitcnt lgkmcnt(3)\n"
        "\tv_mfma_f32_16x16x32_bf16 v[184:187], v[208:211], v[120:123], v[184:187]\n"
        "\tds_read_b128 v[232:235], v6 offset:47104\n"
        "\tv_cndmask_b32 v160, v160, v162, s[60:61]\n"
        "\tv_mfma_f32_16x16x32_bf16 v[188:191], v[208:211], v[124:127], v[188:191]\n"
        "\tds_read_b128 v[236:239], v6 offset:48128\n"
        "\tv_mfma_f32_16x16x32_bf16 v[192:195], v[212:215], v[120:123], v[192:195]\n"
        "\tds_read_b128 v[240:243], v6 offset:49152\n"
        "\tbuffer_load_dwordx4 v245, s[12:15], s96 offen offset:1024 lds\n"
        "\tv_mfma_f32_16x16x32_bf16 v[196:199], v[212:215], v[124:127], v[196:199]\n"
        "\ts_add_u32 s96, s96, 98304\n"
        "\tv_mfma_f32_16x16x32_bf16 v[200:203], v[216:219], v[120:123], v[200:203]\n"
        "\tds_bpermute_b32 v164, v9, v160\n"
        "\tv_mfma_f32_16x16x32_bf16 v[204:207], v[216:219], v[124:127], v[204:207]\n"
        "\tv_accvgpr_read_b32 v160, a248\n"
        "\ts_waitcnt lgkmcnt(4)\n"
        "\tv_mfma_f32_16x16x32_bf16 v[184:187], v[220:223], v[128:131], v[184:187]\n"
        "\tds_read_b128 v[208:211], v6 offset:54272\n"
        "\tbuffer_load_dwordx4 v246, s[12:15], s96 offen offset:2048 lds\n"
        "\tv_mfma_f32_16x16x32_bf16 v[188:191], v[220:223], v[132:135], v[188:191]\n"
        "\tds_read_b128 v[212:215], v6 offset:55296\n"
        "\tv_accvgpr_read_b32 v161, a249\n"
        "\tv_mfma_f32_16x16x32_bf16 v[192:195], v[224:227], v[128:131], v[192:195]\n"
        "\tds_read_b128 v[216:219], v6 offset:56320\n"
        "\tv_accvgpr_read_b32 v162, a250\n"
        "\tv_mfma_f32_16x16x32_bf16 v[196:199], v[224:227], v[132:135], v[196:199]\n"
        "\tv_accvgpr_read_b32 v163, a251\n"
        "\tv_mfma_f32_16x16x32_bf16 v[200:203], v[228:231], v[128:131], v[200:203]\n"
        "\tbuffer_load_dwordx4 v247, s[12:15], s96 offen offset:3072 lds\n"
        "\tv_mfma_f32_16x16x32_bf16 v[204:207], v[228:231], v[132:135], v[204:207]\n"
        "\tv_cndmask_b32 v160, v160, v161, s[58:59]\n"
        "\ts_waitcnt lgkmcnt(4)\n"
        "\tv_mfma_f32_16x16x32_bf16 v[184:187], v[232:235], v[136:139], v[184:187]\n"
        "\tv_cndmask_b32 v162, v162, v163, s[58:59]\n"
        "\tv_mfma_f32_16x16x32_bf16 v[188:191], v[232:235], v[140:143], v[188:191]\n"
        "\tv_cndmask_b32 v160, v160, v162, s[60:61]\n"
        "\tv_mfma_f32_16x16x32_bf16 v[192:195], v[236:239], v[136:139], v[192:195]\n"
        "LWH:\n"
        "\ts_memrealtime s[34:35]\n"
        "\ts_waitcnt lgkmcnt(0)\n"
        "\ts_sub_u32 s35, s31, s34\n"
        "\ts_sub_u32 s35, s35, 1\n"
        "\ts_cmp_lt_u32 s35, 400\n"
        "\ts_cbranch_scc1 LWH\n"
        "\tbuffer_load_dwordx4 v[88:91], v1, s[8:11], s40 offen offset:0 sc1\n"
        "\tv_mfma_f32_16x16x32_bf16 v[196:199], v[236:239], v[140:143], v[196:199]\n"
        "\tds_bpermute_b32 v165, v9, v160\n"
        "\tv_mfma_f32_16x16x32_bf16 v[200:203], v[240:243], v[136:139], v[200:203]\n"
        "\tv_mul_f32 v164, 0x3b000000, v164\n"
        "\tv_mfma_f32_16x16x32_bf16 v[204:207], v[240:243], v[140:143], v[204:207]\n"
        "\ts_waitcnt lgkmcnt(0)\n"
        "\tv_mul_f32 v165, 0x3b000000, v165\n"
        "\tv_mfma_f32_16x16x32_bf16 v[184:187], v[208:211], v[144:147], v[184:187]\n"
        "\tbuffer_load_dwordx4 v[92:95], v1, s[8:11], s45 offen offset:0 sc1\n"
        "\tv_mfma_f32_16x16x32_bf16 v[188:191], v[208:211], v[148:151], v[188:191]\n"
        "\tv_fma_f32 v164, -v20, v20, v164\n"
        "\tv_mfma_f32_16x16x32_bf16 v[192:195], v[212:215], v[144:147], v[192:195]\n"
        "\tv_fma_f32 v165, -v21, v21, v165\n"
        "\tv_mfma_f32_16x16x32_bf16 v[196:199], v[212:215], v[148:151], v[196:199]\n"
        "\tv_max_f32 v164, 0, v164\n"
        "\tv_mfma_f32_16x16x32_bf16 v[200:203], v[216:219], v[144:147], v[200:203]\n"
        "\tbuffer_load_dwordx4 v[96:99], v1, s[8:11], s46 offen offset:0 sc1\n"
        "\tv_mfma_f32_16x16x32_bf16 v[204:207], v[216:219], v[148:151], v[204:207]\n"
        "\tv_max_f32 v165, 0, v165\n"
        "\tv_mfma_f32_16x16x32_bf16 v[184:187], a[208:211], a[228:231], v[184:187]\n"
        "\tv_mfma_f32_16x16x32_bf16 v[188:191], a[208:211], a[232:235], v[188:191]\n"
        "\tbuffer_load_dwordx4 v[100:103], v1, s[8:11], s48 offen offset:0 sc1\n"
        "\tv_mfma_f32_16x16x32_bf16 v[192:195], a[212:215], a[228:231], v[192:195]\n"
        "\tv_mfma_f32_16x16x32_bf16 v[196:199], a[212:215], a[232:235], v[196:199]\n"
        "\tbuffer_load_dwordx4 v[104:107], v1, s[8:11], s49 offen offset:0 sc1\n"
        "\tv_mfma_f32_16x16x32_bf16 v[200:203], a[216:219], a[228:231], v[200:203]\n"
        "\tv_mfma_f32_16x16x32_bf16 v[204:207], a[216:219], a[232:235], v[204:207]\n"
        "\tbuffer_load_dwordx4 v[108:111], v1, s[8:11], s50 offen offset:0 sc1\n"
        "\tv_cvt_pk_bf16_f32 v166, v164, v165\n"
        "\tv_lshlrev_b32 v164, 16, v166\n"
        "\tv_and_b32 v165, s83, v166\n"
        "\tv_add_f32 v164, 0x3727c5ac, v164\n"
        "\tv_add_f32 v165, 0x3727c5ac, v165\n"
        "\tv_cvt_pk_bf16_f32 v166, v164, v165\n"
        "\tv_lshlrev_b32 v164, 16, v166\n"
        "\tv_and_b32 v165, s83, v166\n"
        "\tbuffer_load_dwordx4 v[112:115], v1, s[8:11], s51 offen offset:0 sc1\n"
        "\tv_sqrt_f32 v164, v164\n"
        "\tv_sqrt_f32 v165, v165\n"
        "\ts_nop 0\n"
        "\tv_cvt_pk_bf16_f32 v166, v164, v165\n"
        "\tv_lshlrev_b32 v164, 16, v166\n"
        "\tv_and_b32 v165, s83, v166\n"
        "\tv_rcp_f32 v164, v164\n"
        "\tv_rcp_f32 v165, v165\n"
        "\tds_write_b32 v10, v164\n"
        "\tbuffer_load_dwordx4 v[116:119], v1, s[8:11], s63 offen offset:0 sc1\n"
        "\tds_write_b32 v10, v165 offset:64\n"
        "\tv_cvt_pk_bf16_f32 v184, v184, v185\n"
        "\tv_cvt_pk_bf16_f32 v185, v186, v187\n"
        "\tv_pk_max_i16 v184, v184, s29\n"
        "\tv_pk_max_i16 v185, v185, s29\n"
        "\tv_pk_add_u16 v184, v184, v253\n"
        "\tv_pk_add_u16 v185, v185, v253\n"
        "\tv_cvt_pk_bf16_f32 v186, v192, v193\n"
        "\tbuffer_load_dwordx4 v[120:123], v1, s[8:11], s65 offen offset:0 sc1\n"
        "\tv_cvt_pk_bf16_f32 v187, v194, v195\n"
        "\tv_pk_max_i16 v186, v186, s29\n"
        "\tv_pk_max_i16 v187, v187, s29\n"
        "\tv_pk_add_u16 v186, v186, v253\n"
        "\tv_pk_add_u16 v187, v187, v253\n"
        "\tbuffer_load_dwordx4 v[124:127], v1, s[8:11], s68 offen offset:0 sc1\n"
        "\tbuffer_load_dwordx4 v[128:131], v1, s[8:11], s69 offen offset:0 sc1\n"
        "\tbuffer_load_dwordx4 v[132:135], v1, s[8:11], s97 offen offset:0 sc1\n"
        "\tbuffer_store_dwordx4 v[184:187], v1, s[8:11], s70 offen offset:2048 sc1\n"
        "\tv_cvt_pk_bf16_f32 v188, v188, v189\n"
        "\tv_cvt_pk_bf16_f32 v189, v190, v191\n"
        "\tv_pk_max_i16 v188, v188, s29\n"
        "\tv_pk_max_i16 v189, v189, s29\n"
        "\tv_pk_add_u16 v188, v188, v253\n"
        "\tv_pk_add_u16 v189, v189, v253\n"
        "\tv_cvt_pk_bf16_f32 v190, v196, v197\n"
        "\tv_cvt_pk_bf16_f32 v191, v198, v199\n"
        "\tv_pk_max_i16 v190, v190, s29\n"
        "\tv_pk_max_i16 v191, v191, s29\n"
        "\tv_pk_add_u16 v190, v190, v253\n"
        "\tv_pk_add_u16 v191, v191, v253\n"
        "\tbuffer_store_dwordx4 v[188:191], v1, s[8:11], s71 offen offset:2048 sc1\n"
        "\tv_cvt_pk_bf16_f32 v200, v200, v201\n"
        "\tv_cvt_pk_bf16_f32 v201, v202, v203\n"
        "\tv_pk_max_i16 v200, v200, s29\n"
        "\tv_pk_max_i16 v201, v201, s29\n"
        "\tv_pk_add_u16 v200, v200, v253\n"
        "\tv_pk_add_u16 v201, v201, v253\n"
        "\tv_cvt_pk_bf16_f32 v202, v204, v205\n"
        "\tv_cvt_pk_bf16_f32 v203, v206, v207\n"
        "\tv_pk_max_i16 v202, v202, s29\n"
        "\tv_pk_max_i16 v203, v203, s29\n"
        "\tv_pk_add_u16 v202, v202, v253\n"
        "\tv_pk_add_u16 v203, v203, v253\n"
        "\ts_bitcmp1_b32 s42, 2\n"
        "\ts_cselect_b64 s[58:59], -1, 0\n"
        "\tv_cndmask_b32 v204, v200, v202, s[58:59]\n"
        "\tv_cndmask_b32 v205, v201, v203, s[58:59]\n"
        "\tv_cndmask_b32 v206, v202, v200, s[58:59]\n"
        "\tv_cndmask_b32 v207, v203, v201, s[58:59]\n"
        "\tbuffer_store_dwordx4 v[204:207], v1, s[8:11], s72 offen sc1\n"
        "\ts_waitcnt vmcnt(11)\n"
        "\tv_xor_b32 v22, v88, v252\n"
        "\tv_bitop3_b32 v22, v22, v89, v252 bitop3:0xf6\n"
        "\tv_bitop3_b32 v22, v22, v90, v252 bitop3:0xf6\n"
        "\tv_bitop3_b32 v22, v22, v91, v252 bitop3:0xf6\n"
        "\tv_bitop3_b32 v22, v22, v92, v252 bitop3:0xf6\n"
        "\tv_bitop3_b32 v22, v22, v93, v252 bitop3:0xf6\n"
        "\tv_bitop3_b32 v22, v22, v94, v252 bitop3:0xf6\n"
        "\tv_bitop3_b32 v22, v22, v95, v252 bitop3:0xf6\n"
        "\tv_bitop3_b32 v22, v22, v96, v252 bitop3:0xf6\n"
        "\tv_bitop3_b32 v22, v22, v97, v252 bitop3:0xf6\n"
        "\tv_bitop3_b32 v22, v22, v98, v252 bitop3:0xf6\n"
        "\tv_bitop3_b32 v22, v22, v99, v252 bitop3:0xf6\n"
        "\tv_bitop3_b32 v22, v22, v100, v252 bitop3:0xf6\n"
        "\tv_bitop3_b32 v22, v22, v101, v252 bitop3:0xf6\n"
        "\tv_bitop3_b32 v22, v22, v102, v252 bitop3:0xf6\n"
        "\tv_bitop3_b32 v22, v22, v103, v252 bitop3:0xf6\n"
        "\tv_and_b32 v22, s30, v22\n"
        "\tv_cmp_ne_u32 vcc, 0, v22\n"
        "\ts_cbranch_vccnz LRh0\n"
        "LBh0:\n"
        "\tv_mfma_f32_16x16x32_bf16 v[24:27], a[0:3], v[88:91], 0\n"
        "\tds_read_b64_tr_b16 a[192:193], v248 offset:0\n"
        "\tbuffer_load_dwordx4 v[136:139], v1, s[8:11], s98 offen offset:0 sc1\n"
        "\tv_mfma_f32_16x16x32_bf16 v[28:31], a[4:7], v[88:91], 0\n"
        "\tds_read_b64_tr_b16 a[194:195], v248 offset:2048\n"
        "\ts_lshl_b32 s57, s43, 1\n"
        "\tv_mfma_f32_16x16x32_bf16 v[32:35], a[8:11], v[88:91], 0\n"
        "\tds_read_b64_tr_b16 a[196:197], v249 offset:0\n"
        "\tbuffer_load_dwordx4 v[140:143], v1, s[8:11], s99 offen offset:0 sc1\n"
        "\tv_mfma_f32_16x16x32_bf16 v[36:39], a[12:15], v[88:91], 0\n"
        "\tds_read_b64_tr_b16 a[198:199], v249 offset:2048\n"
        "\ts_add_u32 s57, s57, s62\n"
        "\tv_mfma_f32_16x16x32_bf16 v[24:27], a[32:35], v[92:95], v[24:27]\n"
        "\tds_read_b64_tr_b16 a[200:201], v250 offset:0\n"
        "\tbuffer_load_dwordx4 v[144:147], v1, s[8:11], s74 offen offset:0 sc1\n"
        "\tv_mfma_f32_16x16x32_bf16 v[28:31], a[36:39], v[92:95], v[28:31]\n"
        "\tbuffer_load_dwordx4 v[148:151], v1, s[8:11], s76 offen offset:0 sc1\n"
        "\ts_waitcnt vmcnt(11)\n"
        "\tv_mfma_f32_16x16x32_bf16 v[32:35], a[40:43], v[92:95], v[32:35]\n"
        "\tv_xor_b32 v22, v104, v252\n"
        "\tv_bitop3_b32 v22, v22, v105, v252 bitop3:0xf6\n"
        "\tv_mfma_f32_16x16x32_bf16 v[36:39], a[44:47], v[92:95], v[36:39]\n"
        "\tv_bitop3_b32 v22, v22, v106, v252 bitop3:0xf6\n"
        "\tv_bitop3_b32 v22, v22, v107, v252 bitop3:0xf6\n"
        "\tv_mfma_f32_16x16x32_bf16 v[24:27], a[64:67], v[96:99], v[24:27]\n"
        "\tv_bitop3_b32 v22, v22, v108, v252 bitop3:0xf6\n"
        "\tv_bitop3_b32 v22, v22, v109, v252 bitop3:0xf6\n"
        "\tv_mfma_f32_16x16x32_bf16 v[28:31], a[68:71], v[96:99], v[28:31]\n"
        "\tv_bitop3_b32 v22, v22, v110, v252 bitop3:0xf6\n"
        "\tv_bitop3_b32 v22, v22, v111, v252 bitop3:0xf6\n"
        "\tv_mfma_f32_16x16x32_bf16 v[32:35], a[72:75], v[96:99], v[32:35]\n"
        "\tv_bitop3_b32 v22, v22, v112, v252 bitop3:0xf6\n"
        "\tv_bitop3_b32 v22, v22, v113, v252 bitop3:0xf6\n"
        "\tv_mfma_f32_16x16x32_bf16 v[36:39], a[76:79], v[96:99], v[36:39]\n"
        "\tv_bitop3_b32 v22, v22, v114, v252 bitop3:0xf6\n"
        "\tv_bitop3_b32 v22, v22, v115, v252 bitop3:0xf6\n"
        "\tv_mfma_f32_16x16x32_bf16 v[24:27], a[96:99], v[100:103], v[24:27]\n"
        "\tv_bitop3_b32 v22, v22, v116, v252 bitop3:0xf6\n"
        "\tv_bitop3_b32 v22, v22, v117, v252 bitop3:0xf6\n"
        "\tv_mfma_f32_16x16x32_bf16 v[28:31], a[100:103], v[100:103], v[28:31]\n"
        "\tv_bitop3_b32 v22, v22, v118, v252 bitop3:0xf6\n"
        "\tv_bitop3_b32 v22, v22, v119, v252 bitop3:0xf6\n"
        "\tv_mfma_f32_16x16x32_bf16 v[32:35], a[104:107], v[100:103], v[32:35]\n"
        "\tv_and_b32 v22, s30, v22\n"
        "\tv_cmp_ne_u32 vcc, 0, v22\n"
        "\tv_mfma_f32_16x16x32_bf16 v[36:39], a[108:111], v[100:103], v[36:39]\n"
        "\tds_read_b64_tr_b16 a[202:203], v250 offset:2048\n"
        "\ts_mul_i32 s57, s57, 33536\n"
        "\ts_cbranch_vccnz LRh1\n"
        "LBh1:\n"
        "\tv_mfma_f32_16x16x32_bf16 v[40:43], a[0:3], v[104:107], 0\n"
        "\tds_read_b64_tr_b16 a[204:205], v251 offset:0\n"
        "\tbuffer_load_dwordx4 v[88:91], v1, s[8:11], s40 offen offset:1024 sc1\n"
        "\tv_mfma_f32_16x16x32_bf16 v[44:47], a[4:7], v[104:107], 0\n"
        "\tds_read_b64_tr_b16 a[206:207], v251 offset:2048\n"
        "\ts_lshl_b32 s58, s44, 12\n"
        "\tv_mfma_f32_16x16x32_bf16 v[48:51], a[8:11], v[104:107], 0\n"
        "\tds_read_b64_tr_b16 a[208:209], v248 offset:4096\n"
        "\tbuffer_load_dwordx4 v[92:95], v1, s[8:11], s45 offen offset:1024 sc1\n"
        "\tv_mfma_f32_16x16x32_bf16 v[52:55], a[12:15], v[104:107], 0\n"
        "\tds_read_b64_tr_b16 a[210:211], v248 offset:6144\n"
        "\ts_add_u32 s57, s57, s58\n"
        "\tv_mfma_f32_16x16x32_bf16 v[40:43], a[32:35], v[108:111], v[40:43]\n"
        "\tds_read_b64_tr_b16 a[212:213], v249 offset:4096\n"
        "\tbuffer_load_dwordx4 v[96:99], v1, s[8:11], s46 offen offset:1024 sc1\n"
        "\tv_mfma_f32_16x16x32_bf16 v[44:47], a[36:39], v[108:111], v[44:47]\n"
        "\tbuffer_load_dwordx4 v[100:103], v1, s[8:11], s48 offen offset:1024 sc1\n"
        "\ts_waitcnt vmcnt(11)\n"
        "\tv_mfma_f32_16x16x32_bf16 v[48:51], a[40:43], v[108:111], v[48:51]\n"
        "\tv_xor_b32 v22, v120, v252\n"
        "\tv_bitop3_b32 v22, v22, v121, v252 bitop3:0xf6\n"
        "\tv_mfma_f32_16x16x32_bf16 v[52:55], a[44:47], v[108:111], v[52:55]\n"
        "\tv_bitop3_b32 v22, v22, v122, v252 bitop3:0xf6\n"
        "\tv_bitop3_b32 v22, v22, v123, v252 bitop3:0xf6\n"
        "\tv_mfma_f32_16x16x32_bf16 v[40:43], a[64:67], v[112:115], v[40:43]\n"
        "\tv_bitop3_b32 v22, v22, v124, v252 bitop3:0xf6\n"
        "\tv_bitop3_b32 v22, v22, v125, v252 bitop3:0xf6\n"
        "\tv_mfma_f32_16x16x32_bf16 v[44:47], a[68:71], v[112:115], v[44:47]\n"
        "\tv_bitop3_b32 v22, v22, v126, v252 bitop3:0xf6\n"
        "\tv_bitop3_b32 v22, v22, v127, v252 bitop3:0xf6\n"
        "\tv_mfma_f32_16x16x32_bf16 v[48:51], a[72:75], v[112:115], v[48:51]\n"
        "\tv_bitop3_b32 v22, v22, v128, v252 bitop3:0xf6\n"
        "\tv_bitop3_b32 v22, v22, v129, v252 bitop3:0xf6\n"
        "\tv_mfma_f32_16x16x32_bf16 v[52:55], a[76:79], v[112:115], v[52:55]\n"
        "\tv_bitop3_b32 v22, v22, v130, v252 bitop3:0xf6\n"
        "\tv_bitop3_b32 v22, v22, v131, v252 bitop3:0xf6\n"
        "\tv_mfma_f32_16x16x32_bf16 v[40:43], a[96:99], v[116:119], v[40:43]\n"
        "\tv_bitop3_b32 v22, v22, v132, v252 bitop3:0xf6\n"
        "\tv_bitop3_b32 v22, v22, v133, v252 bitop3:0xf6\n"
        "\tv_mfma_f32_16x16x32_bf16 v[44:47], a[100:103], v[116:119], v[44:47]\n"
        "\tv_bitop3_b32 v22, v22, v134, v252 bitop3:0xf6\n"
        "\tv_bitop3_b32 v22, v22, v135, v252 bitop3:0xf6\n"
        "\tv_mfma_f32_16x16x32_bf16 v[48:51], a[104:107], v[116:119], v[48:51]\n"
        "\tv_and_b32 v22, s30, v22\n"
        "\tv_cmp_ne_u32 vcc, 0, v22\n"
        "\tv_mfma_f32_16x16x32_bf16 v[52:55], a[108:111], v[116:119], v[52:55]\n"
        "\tds_read_b64_tr_b16 a[214:215], v249 offset:6144\n"
        "\ts_add_u32 s57, s57, 0x2c0000\n"
        "\ts_cbranch_vccnz LRh2\n"
        "LBh2:\n"
        "\tv_mfma_f32_16x16x32_bf16 v[56:59], a[0:3], v[120:123], 0\n"
        "\tds_read_b64_tr_b16 a[216:217], v250 offset:4096\n"
        "\tbuffer_load_dwordx4 v[104:107], v1, s[8:11], s49 offen offset:1024 sc1\n"
        "\tv_mfma_f32_16x16x32_bf16 v[60:63], a[4:7], v[120:123], 0\n"
        "\tds_read_b64_tr_b16 a[218:219], v250 offset:6144\n"
        "\ts_add_u32 m0, s67, 8192\n"
        "\tv_mfma_f32_16x16x32_bf16 v[64:67], a[8:11], v[120:123], 0\n"
        "\tds_read_b64_tr_b16 a[220:221], v251 offset:4096\n"
        "\tbuffer_load_dwordx4 v[108:111], v1, s[8:11], s50 offen offset:1024 sc1\n"
        "\tv_mfma_f32_16x16x32_bf16 v[68:71], a[12:15], v[120:123], 0\n"
        "\tds_read_b64_tr_b16 a[222:223], v251 offset:6144\n"
        "\ts_add_u32 s58, s57, 1024\n"
        "\tv_mfma_f32_16x16x32_bf16 v[56:59], a[32:35], v[124:127], v[56:59]\n"
        "\tbuffer_load_dwordx4 v[112:115], v1, s[8:11], s51 offen offset:1024 sc1\n"
        "\tv_mfma_f32_16x16x32_bf16 v[60:63], a[36:39], v[124:127], v[60:63]\n"
        "\tbuffer_load_dwordx4 v[116:119], v1, s[8:11], s63 offen offset:1024 sc1\n"
        "\ts_waitcnt vmcnt(8)\n"
        "\tv_mfma_f32_16x16x32_bf16 v[64:67], a[40:43], v[124:127], v[64:67]\n"
        "\tv_xor_b32 v22, v136, v252\n"
        "\tv_bitop3_b32 v22, v22, v137, v252 bitop3:0xf6\n"
        "\tv_mfma_f32_16x16x32_bf16 v[68:71], a[44:47], v[124:127], v[68:71]\n"
        "\tv_bitop3_b32 v22, v22, v138, v252 bitop3:0xf6\n"
        "\tv_bitop3_b32 v22, v22, v139, v252 bitop3:0xf6\n"
        "\tv_mfma_f32_16x16x32_bf16 v[56:59], a[64:67], v[128:131], v[56:59]\n"
        "\tv_bitop3_b32 v22, v22, v140, v252 bitop3:0xf6\n"
        "\tv_bitop3_b32 v22, v22, v141, v252 bitop3:0xf6\n"
        "\tv_mfma_f32_16x16x32_bf16 v[60:63], a[68:71], v[128:131], v[60:63]\n"
        "\tv_bitop3_b32 v22, v22, v142, v252 bitop3:0xf6\n"
        "\tv_bitop3_b32 v22, v22, v143, v252 bitop3:0xf6\n"
        "\tv_mfma_f32_16x16x32_bf16 v[64:67], a[72:75], v[128:131], v[64:67]\n"
        "\tv_bitop3_b32 v22, v22, v144, v252 bitop3:0xf6\n"
        "\tv_bitop3_b32 v22, v22, v145, v252 bitop3:0xf6\n"
        "\tv_mfma_f32_16x16x32_bf16 v[68:71], a[76:79], v[128:131], v[68:71]\n"
        "\tv_bitop3_b32 v22, v22, v146, v252 bitop3:0xf6\n"
        "\tv_bitop3_b32 v22, v22, v147, v252 bitop3:0xf6\n"
        "\tv_mfma_f32_16x16x32_bf16 v[56:59], a[96:99], v[132:135], v[56:59]\n"
        "\tv_bitop3_b32 v22, v22, v148, v252 bitop3:0xf6\n"
        "\tv_bitop3_b32 v22, v22, v149, v252 bitop3:0xf6\n"
        "\tv_mfma_f32_16x16x32_bf16 v[60:63], a[100:103], v[132:135], v[60:63]\n"
        "\tv_bitop3_b32 v22, v22, v150, v252 bitop3:0xf6\n"
        "\tv_bitop3_b32 v22, v22, v151, v252 bitop3:0xf6\n"
        "\tv_mfma_f32_16x16x32_bf16 v[64:67], a[104:107], v[132:135], v[64:67]\n"
        "\tv_and_b32 v22, s30, v22\n"
        "\tv_cmp_ne_u32 vcc, 0, v22\n"
        "\tv_mfma_f32_16x16x32_bf16 v[68:71], a[108:111], v[132:135], v[68:71]\n"
        "\ts_cbranch_vccnz LRh3\n"
        "LBh3:\n"
        "\tv_mfma_f32_16x16x32_bf16 v[72:75], a[0:3], v[136:139], 0\n"
        "\tbuffer_load_dwordx4 v[120:123], v1, s[8:11], s65 offen offset:1024 sc1\n"
        "\tv_mfma_f32_16x16x32_bf16 v[76:79], a[4:7], v[136:139], 0\n"
        "\tv_mfma_f32_16x16x32_bf16 v[80:83], a[8:11], v[136:139], 0\n"
        "\tbuffer_load_dwordx4 v[124:127], v1, s[8:11], s68 offen offset:1024 sc1\n"
        "\tv_mfma_f32_16x16x32_bf16 v[84:87], a[12:15], v[136:139], 0\n"
        "\tv_mfma_f32_16x16x32_bf16 v[72:75], a[32:35], v[140:143], v[72:75]\n"
        "\tbuffer_load_dwordx4 v[128:131], v1, s[8:11], s69 offen offset:1024 sc1\n"
        "\tv_mfma_f32_16x16x32_bf16 v[76:79], a[36:39], v[140:143], v[76:79]\n"
        "\tbuffer_load_dwordx4 v[132:135], v1, s[8:11], s97 offen offset:1024 sc1\n"
        "\ts_waitcnt vmcnt(8)\n"
        "\tv_mfma_f32_16x16x32_bf16 v[80:83], a[40:43], v[140:143], v[80:83]\n"
        "\tv_xor_b32 v22, v88, v252\n"
        "\tv_bitop3_b32 v22, v22, v89, v252 bitop3:0xf6\n"
        "\tv_mfma_f32_16x16x32_bf16 v[84:87], a[44:47], v[140:143], v[84:87]\n"
        "\tv_bitop3_b32 v22, v22, v90, v252 bitop3:0xf6\n"
        "\tv_bitop3_b32 v22, v22, v91, v252 bitop3:0xf6\n"
        "\tv_mfma_f32_16x16x32_bf16 v[72:75], a[64:67], v[144:147], v[72:75]\n"
        "\tv_bitop3_b32 v22, v22, v92, v252 bitop3:0xf6\n"
        "\tv_bitop3_b32 v22, v22, v93, v252 bitop3:0xf6\n"
        "\tv_mfma_f32_16x16x32_bf16 v[76:79], a[68:71], v[144:147], v[76:79]\n"
        "\tv_bitop3_b32 v22, v22, v94, v252 bitop3:0xf6\n"
        "\tv_bitop3_b32 v22, v22, v95, v252 bitop3:0xf6\n"
        "\tv_mfma_f32_16x16x32_bf16 v[80:83], a[72:75], v[144:147], v[80:83]\n"
        "\tv_bitop3_b32 v22, v22, v96, v252 bitop3:0xf6\n"
        "\tv_bitop3_b32 v22, v22, v97, v252 bitop3:0xf6\n"
        "\tv_mfma_f32_16x16x32_bf16 v[84:87], a[76:79], v[144:147], v[84:87]\n"
        "\tv_bitop3_b32 v22, v22, v98, v252 bitop3:0xf6\n"
        "\tv_bitop3_b32 v22, v22, v99, v252 bitop3:0xf6\n"
        "\tv_mfma_f32_16x16x32_bf16 v[72:75], a[96:99], v[148:151], v[72:75]\n"
        "\tv_bitop3_b32 v22, v22, v100, v252 bitop3:0xf6\n"
        "\tv_bitop3_b32 v22, v22, v101, v252 bitop3:0xf6\n"
        "\tv_mfma_f32_16x16x32_bf16 v[76:79], a[100:103], v[148:151], v[76:79]\n"
        "\tv_bitop3_b32 v22, v22, v102, v252 bitop3:0xf6\n"
        "\tv_bitop3_b32 v22, v22, v103, v252 bitop3:0xf6\n"
        "\tv_mfma_f32_16x16x32_bf16 v[80:83], a[104:107], v[148:151], v[80:83]\n"
        "\tv_and_b32 v22, s30, v22\n"
        "\tv_cmp_ne_u32 vcc, 0, v22\n"
        "\tv_mfma_f32_16x16x32_bf16 v[84:87], a[108:111], v[148:151], v[84:87]\n"
        "\ts_cbranch_vccnz LRh4\n"
        "LBh4:\n"
        "\tv_mfma_f32_16x16x32_bf16 v[24:27], a[16:19], v[88:91], v[24:27]\n"
        "\tbuffer_load_dwordx4 v[136:139], v1, s[8:11], s78 offen offset:0 sc1\n"
        "\tv_mfma_f32_16x16x32_bf16 v[28:31], a[20:23], v[88:91], v[28:31]\n"
        "\tv_mfma_f32_16x16x32_bf16 v[32:35], a[24:27], v[88:91], v[32:35]\n"
        "\tbuffer_load_dwordx4 v[140:143], v1, s[8:11], s78 offen offset:1024 sc1\n"
        "\tv_mfma_f32_16x16x32_bf16 v[36:39], a[28:31], v[88:91], v[36:39]\n"
        "\tv_mfma_f32_16x16x32_bf16 v[24:27], a[48:51], v[92:95], v[24:27]\n"
        "\tbuffer_load_dwordx4 v[144:147], v1, s[8:11], s78 offen offset:2048 sc1\n"
        "\tv_mfma_f32_16x16x32_bf16 v[28:31], a[52:55], v[92:95], v[28:31]\n"
        "\tbuffer_load_dwordx4 v[148:151], v1, s[8:11], s78 offen offset:3072 sc1\n"
        "\ts_waitcnt vmcnt(8)\n"
        "\tv_mfma_f32_16x16x32_bf16 v[32:35], a[56:59], v[92:95], v[32:35]\n"
        "\tv_xor_b32 v22, v104, v252\n"
        "\tv_bitop3_b32 v22, v22, v105, v252 bitop3:0xf6\n"
        "\tv_mfma_f32_16x16x32_bf16 v[36:39], a[60:63], v[92:95], v[36:39]\n"
        "\tv_bitop3_b32 v22, v22, v106, v252 bitop3:0xf6\n"
        "\tv_bitop3_b32 v22, v22, v107, v252 bitop3:0xf6\n"
        "\tv_mfma_f32_16x16x32_bf16 v[24:27], a[80:83], v[96:99], v[24:27]\n"
        "\tv_bitop3_b32 v22, v22, v108, v252 bitop3:0xf6\n"
        "\tv_bitop3_b32 v22, v22, v109, v252 bitop3:0xf6\n"
        "\tv_mfma_f32_16x16x32_bf16 v[28:31], a[84:87], v[96:99], v[28:31]\n"
        "\tv_bitop3_b32 v22, v22, v110, v252 bitop3:0xf6\n"
        "\tv_bitop3_b32 v22, v22, v111, v252 bitop3:0xf6\n"
        "\tv_mfma_f32_16x16x32_bf16 v[32:35], a[88:91], v[96:99], v[32:35]\n"
        "\tv_bitop3_b32 v22, v22, v112, v252 bitop3:0xf6\n"
        "\tv_bitop3_b32 v22, v22, v113, v252 bitop3:0xf6\n"
        "\tv_mfma_f32_16x16x32_bf16 v[36:39], a[92:95], v[96:99], v[36:39]\n"
        "\tv_bitop3_b32 v22, v22, v114, v252 bitop3:0xf6\n"
        "\tv_bitop3_b32 v22, v22, v115, v252 bitop3:0xf6\n"
        "\tv_mfma_f32_16x16x32_bf16 v[24:27], a[112:115], v[100:103], v[24:27]\n"
        "\tv_bitop3_b32 v22, v22, v116, v252 bitop3:0xf6\n"
        "\tv_bitop3_b32 v22, v22, v117, v252 bitop3:0xf6\n"
        "\tv_mfma_f32_16x16x32_bf16 v[28:31], a[116:119], v[100:103], v[28:31]\n"
        "\tv_bitop3_b32 v22, v22, v118, v252 bitop3:0xf6\n"
        "\tv_bitop3_b32 v22, v22, v119, v252 bitop3:0xf6\n"
        "\tv_mfma_f32_16x16x32_bf16 v[32:35], a[120:123], v[100:103], v[32:35]\n"
        "\tv_and_b32 v22, s30, v22\n"
        "\tv_cmp_ne_u32 vcc, 0, v22\n"
        "\tv_mfma_f32_16x16x32_bf16 v[36:39], a[124:127], v[100:103], v[36:39]\n"
        "\ts_cbranch_vccnz LRh5\n"
        "LBh5:\n"
        "\tv_mfma_f32_16x16x32_bf16 v[40:43], a[16:19], v[104:107], v[40:43]\n"
        "\tbuffer_load_dwordx4 v[88:91], v1, s[8:11], s40 offen offset:2048 sc1\n"
        "\tv_mfma_f32_16x16x32_bf16 v[44:47], a[20:23], v[104:107], v[44:47]\n"
        "\tv_mfma_f32_16x16x32_bf16 v[48:51], a[24:27], v[104:107], v[48:51]\n"
        "\tbuffer_load_dwordx4 v[92:95], v1, s[8:11], s45 offen offset:2048 sc1\n"
        "\tv_mfma_f32_16x16x32_bf16 v[52:55], a[28:31], v[104:107], v[52:55]\n"
        "\tv_mfma_f32_16x16x32_bf16 v[40:43], a[48:51], v[108:111], v[40:43]\n"
        "\tbuffer_load_dwordx4 v[96:99], v1, s[8:11], s46 offen offset:2048 sc1\n"
        "\tv_mfma_f32_16x16x32_bf16 v[44:47], a[52:55], v[108:111], v[44:47]\n"
        "\tbuffer_load_dwordx4 v[100:103], v1, s[8:11], s48 offen offset:2048 sc1\n"
        "\ts_waitcnt vmcnt(8)\n"
        "\tv_mfma_f32_16x16x32_bf16 v[48:51], a[56:59], v[108:111], v[48:51]\n"
        "\tv_xor_b32 v22, v120, v252\n"
        "\tv_bitop3_b32 v22, v22, v121, v252 bitop3:0xf6\n"
        "\tv_mfma_f32_16x16x32_bf16 v[52:55], a[60:63], v[108:111], v[52:55]\n"
        "\tv_bitop3_b32 v22, v22, v122, v252 bitop3:0xf6\n"
        "\tv_bitop3_b32 v22, v22, v123, v252 bitop3:0xf6\n"
        "\tv_mfma_f32_16x16x32_bf16 v[40:43], a[80:83], v[112:115], v[40:43]\n"
        "\tv_bitop3_b32 v22, v22, v124, v252 bitop3:0xf6\n"
        "\tv_bitop3_b32 v22, v22, v125, v252 bitop3:0xf6\n"
        "\tv_mfma_f32_16x16x32_bf16 v[44:47], a[84:87], v[112:115], v[44:47]\n"
        "\tv_bitop3_b32 v22, v22, v126, v252 bitop3:0xf6\n"
        "\tv_bitop3_b32 v22, v22, v127, v252 bitop3:0xf6\n"
        "\tv_mfma_f32_16x16x32_bf16 v[48:51], a[88:91], v[112:115], v[48:51]\n"
        "\tv_bitop3_b32 v22, v22, v128, v252 bitop3:0xf6\n"
        "\tv_bitop3_b32 v22, v22, v129, v252 bitop3:0xf6\n"
        "\tv_mfma_f32_16x16x32_bf16 v[52:55], a[92:95], v[112:115], v[52:55]\n"
        "\tv_bitop3_b32 v22, v22, v130, v252 bitop3:0xf6\n"
        "\tv_bitop3_b32 v22, v22, v131, v252 bitop3:0xf6\n"
        "\tv_mfma_f32_16x16x32_bf16 v[40:43], a[112:115], v[116:119], v[40:43]\n"
        "\tv_bitop3_b32 v22, v22, v132, v252 bitop3:0xf6\n"
        "\tv_bitop3_b32 v22, v22, v133, v252 bitop3:0xf6\n"
        "\tv_mfma_f32_16x16x32_bf16 v[44:47], a[116:119], v[116:119], v[44:47]\n"
        "\tv_bitop3_b32 v22, v22, v134, v252 bitop3:0xf6\n"
        "\tv_bitop3_b32 v22, v22, v135, v252 bitop3:0xf6\n"
        "\tv_mfma_f32_16x16x32_bf16 v[48:51], a[120:123], v[116:119], v[48:51]\n"
        "\tv_and_b32 v22, s30, v22\n"
        "\tv_cmp_ne_u32 vcc, 0, v22\n"
        "\tv_mfma_f32_16x16x32_bf16 v[52:55], a[124:127], v[116:119], v[52:55]\n"
        "\ts_cbranch_vccnz LRh6\n"
        "LBh6:\n"
        "\tv_mfma_f32_16x16x32_bf16 v[56:59], a[16:19], v[120:123], v[56:59]\n"
        "\ts_waitcnt lgkmcnt(0)\n"
        "\ts_barrier\n"
        "\tds_read_b32 v19, v11\n"
        "\tv_mfma_f32_16x16x32_bf16 v[60:63], a[20:23], v[120:123], v[60:63]\n"
        "\tv_mfma_f32_16x16x32_bf16 v[64:67], a[24:27], v[120:123], v[64:67]\n"
        "\tbuffer_load_dwordx4 v[104:107], v1, s[8:11], s49 offen offset:2048 sc1\n"
        "\tv_mfma_f32_16x16x32_bf16 v[68:71], a[28:31], v[120:123], v[68:71]\n"
        "\tbuffer_load_dwordx4 v[108:111], v1, s[8:11], s50 offen offset:2048 sc1\n"
        "\tv_mfma_f32_16x16x32_bf16 v[56:59], a[48:51], v[124:127], v[56:59]\n"
        "\tbuffer_load_dwordx4 v[112:115], v1, s[8:11], s51 offen offset:2048 sc1\n"
        "\tv_mfma_f32_16x16x32_bf16 v[60:63], a[52:55], v[124:127], v[60:63]\n"
        "\tbuffer_load_dwordx4 v[116:119], v1, s[8:11], s63 offen offset:2048 sc1\n"
        "\ts_waitcnt vmcnt(8)\n"
        "\tv_mfma_f32_16x16x32_bf16 v[64:67], a[56:59], v[124:127], v[64:67]\n"
        "\tv_xor_b32 v22, v136, v252\n"
        "\tv_bitop3_b32 v22, v22, v137, v252 bitop3:0xf6\n"
        "\tv_mfma_f32_16x16x32_bf16 v[68:71], a[60:63], v[124:127], v[68:71]\n"
        "\tv_bitop3_b32 v22, v22, v138, v252 bitop3:0xf6\n"
        "\tv_bitop3_b32 v22, v22, v139, v252 bitop3:0xf6\n"
        "\tv_mfma_f32_16x16x32_bf16 v[56:59], a[80:83], v[128:131], v[56:59]\n"
        "\tv_bitop3_b32 v22, v22, v140, v252 bitop3:0xf6\n"
        "\tv_bitop3_b32 v22, v22, v141, v252 bitop3:0xf6\n"
        "\tv_mfma_f32_16x16x32_bf16 v[60:63], a[84:87], v[128:131], v[60:63]\n"
        "\tv_bitop3_b32 v22, v22, v142, v252 bitop3:0xf6\n"
        "\tv_bitop3_b32 v22, v22, v143, v252 bitop3:0xf6\n"
        "\tv_mfma_f32_16x16x32_bf16 v[64:67], a[88:91], v[128:131], v[64:67]\n"
        "\tv_bitop3_b32 v22, v22, v144, v252 bitop3:0xf6\n"
        "\tv_bitop3_b32 v22, v22, v145, v252 bitop3:0xf6\n"
        "\tv_mfma_f32_16x16x32_bf16 v[68:71], a[92:95], v[128:131], v[68:71]\n"
        "\tv_bitop3_b32 v22, v22, v146, v252 bitop3:0xf6\n"
        "\tv_bitop3_b32 v22, v22, v147, v252 bitop3:0xf6\n"
        "\tv_mfma_f32_16x16x32_bf16 v[56:59], a[112:115], v[132:135], v[56:59]\n"
        "\tv_bitop3_b32 v22, v22, v148, v252 bitop3:0xf6\n"
        "\tv_bitop3_b32 v22, v22, v149, v252 bitop3:0xf6\n"
        "\tv_mfma_f32_16x16x32_bf16 v[60:63], a[116:119], v[132:135], v[60:63]\n"
        "\tv_bitop3_b32 v22, v22, v150, v252 bitop3:0xf6\n"
        "\tv_bitop3_b32 v22, v22, v151, v252 bitop3:0xf6\n"
        "\tv_mfma_f32_16x16x32_bf16 v[64:67], a[120:123], v[132:135], v[64:67]\n"
        "\tv_and_b32 v22, s30, v22\n"
        "\tv_cmp_ne_u32 vcc, 0, v22\n"
        "\tv_mfma_f32_16x16x32_bf16 v[68:71], a[124:127], v[132:135], v[68:71]\n"
        "\ts_cbranch_vccnz LRh7\n"
        "LBh7:\n"
        "\tv_swap_b32 v138, v140\n"
        "\tv_swap_b32 v139, v141\n"
        "\tv_swap_b32 v146, v148\n"
        "\tv_swap_b32 v147, v149\n"
        "\tv_mfma_f32_16x16x32_bf16 v[24:27], a[192:195], v[136:139], v[24:27]\n"
        "\tbuffer_load_dwordx4 v[120:123], v1, s[8:11], s79 offen offset:0 sc1\n"
        "\tv_mfma_f32_16x16x32_bf16 v[28:31], a[196:199], v[136:139], v[28:31]\n"
        "\tv_mfma_f32_16x16x32_bf16 v[32:35], a[200:203], v[136:139], v[32:35]\n"
        "\tbuffer_load_dwordx4 v[124:127], v1, s[8:11], s79 offen offset:1024 sc1\n"
        "\tv_mfma_f32_16x16x32_bf16 v[36:39], a[204:207], v[136:139], v[36:39]\n"
        "\tbuffer_load_dwordx4 v[128:131], v1, s[8:11], s79 offen offset:2048 sc1\n"
        "\tv_mfma_f32_16x16x32_bf16 v[40:43], a[192:195], v[140:143], v[40:43]\n"
        "\tbuffer_load_dwordx4 v[132:135], v1, s[8:11], s79 offen offset:3072 sc1\n"
        "\tv_mfma_f32_16x16x32_bf16 v[44:47], a[196:199], v[140:143], v[44:47]\n"
        "\tbuffer_load_dwordx4 v12, s[8:11], s57 offen sc1 lds\n"
        "\ts_waitcnt vmcnt(9)\n"
        "\tv_mfma_f32_16x16x32_bf16 v[48:51], a[200:203], v[140:143], v[48:51]\n"
        "\tv_xor_b32 v22, v88, v252\n"
        "\tv_bitop3_b32 v22, v22, v89, v252 bitop3:0xf6\n"
        "\tv_mfma_f32_16x16x32_bf16 v[52:55], a[204:207], v[140:143], v[52:55]\n"
        "\tv_bitop3_b32 v22, v22, v90, v252 bitop3:0xf6\n"
        "\tv_bitop3_b32 v22, v22, v91, v252 bitop3:0xf6\n"
        "\tv_mfma_f32_16x16x32_bf16 v[24:27], a[208:211], v[144:147], v[24:27]\n"
        "\tv_bitop3_b32 v22, v22, v92, v252 bitop3:0xf6\n"
        "\tv_bitop3_b32 v22, v22, v93, v252 bitop3:0xf6\n"
        "\tv_mfma_f32_16x16x32_bf16 v[28:31], a[212:215], v[144:147], v[28:31]\n"
        "\tv_bitop3_b32 v22, v22, v94, v252 bitop3:0xf6\n"
        "\tv_bitop3_b32 v22, v22, v95, v252 bitop3:0xf6\n"
        "\tv_mfma_f32_16x16x32_bf16 v[32:35], a[216:219], v[144:147], v[32:35]\n"
        "\tv_bitop3_b32 v22, v22, v96, v252 bitop3:0xf6\n"
        "\tv_bitop3_b32 v22, v22, v97, v252 bitop3:0xf6\n"
        "\tv_mfma_f32_16x16x32_bf16 v[36:39], a[220:223], v[144:147], v[36:39]\n"
        "\tv_bitop3_b32 v22, v22, v98, v252 bitop3:0xf6\n"
        "\tv_bitop3_b32 v22, v22, v99, v252 bitop3:0xf6\n"
        "\tv_mfma_f32_16x16x32_bf16 v[40:43], a[208:211], v[148:151], v[40:43]\n"
        "\tv_bitop3_b32 v22, v22, v100, v252 bitop3:0xf6\n"
        "\tv_bitop3_b32 v22, v22, v101, v252 bitop3:0xf6\n"
        "\tv_mfma_f32_16x16x32_bf16 v[44:47], a[212:215], v[148:151], v[44:47]\n"
        "\tv_bitop3_b32 v22, v22, v102, v252 bitop3:0xf6\n"
        "\tv_bitop3_b32 v22, v22, v103, v252 bitop3:0xf6\n"
        "\tv_mfma_f32_16x16x32_bf16 v[48:51], a[216:219], v[148:151], v[48:51]\n"
        "\tv_and_b32 v22, s30, v22\n"
        "\tv_cmp_ne_u32 vcc, 0, v22\n"
        "\tv_mfma_f32_16x16x32_bf16 v[52:55], a[220:223], v[148:151], v[52:55]\n"
        "\ts_cbranch_vccnz LRh8\n"
        "LBh8:\n"
        "\tv_mfma_f32_16x16x32_bf16 v[24:27], a[128:131], v[88:91], v[24:27]\n"
        "\tbuffer_load_dwordx4 v[136:139], v1, s[8:11], s65 offen offset:2048 sc1\n"
        "\ts_waitcnt lgkmcnt(0)\n"
        "\tv_mul_f32 v19, v19, v254\n"
        "\tv_mfma_f32_16x16x32_bf16 v[28:31], a[132:135], v[88:91], v[28:31]\n"
        "\tv_mfma_f32_16x16x32_bf16 v[32:35], a[136:139], v[88:91], v[32:35]\n"
        "\tbuffer_load_dwordx4 v[140:143], v1, s[8:11], s68 offen offset:2048 sc1\n"
        "\tv_mfma_f32_16x16x32_bf16 v[36:39], a[140:143], v[88:91], v[36:39]\n"
        "\tbuffer_load_dwordx4 v[144:147], v1, s[8:11], s69 offen offset:2048 sc1\n"
        "\tv_mfma_f32_16x16x32_bf16 v[24:27], a[144:147], v[92:95], v[24:27]\n"
        "\tbuffer_load_dwordx4 v[148:151], v1, s[8:11], s97 offen offset:2048 sc1\n"
        "\tv_mfma_f32_16x16x32_bf16 v[28:31], a[148:151], v[92:95], v[28:31]\n"
        "\tbuffer_load_dwordx4 v12, s[8:11], s58 offen offset:1024 sc1 lds\n"
        "\ts_waitcnt vmcnt(10)\n"
        "\tv_mfma_f32_16x16x32_bf16 v[32:35], a[152:155], v[92:95], v[32:35]\n"
        "\tv_xor_b32 v22, v104, v252\n"
        "\tv_bitop3_b32 v22, v22, v105, v252 bitop3:0xf6\n"
        "\tv_mfma_f32_16x16x32_bf16 v[36:39], a[156:159], v[92:95], v[36:39]\n"
        "\tv_bitop3_b32 v22, v22, v106, v252 bitop3:0xf6\n"
        "\tv_bitop3_b32 v22, v22, v107, v252 bitop3:0xf6\n"
        "\tv_mfma_f32_16x16x32_bf16 v[24:27], a[160:163], v[96:99], v[24:27]\n"
        "\tv_bitop3_b32 v22, v22, v108, v252 bitop3:0xf6\n"
        "\tv_bitop3_b32 v22, v22, v109, v252 bitop3:0xf6\n"
        "\tv_mfma_f32_16x16x32_bf16 v[28:31], a[164:167], v[96:99], v[28:31]\n"
        "\tv_bitop3_b32 v22, v22, v110, v252 bitop3:0xf6\n"
        "\tv_bitop3_b32 v22, v22, v111, v252 bitop3:0xf6\n"
        "\tv_mfma_f32_16x16x32_bf16 v[32:35], a[168:171], v[96:99], v[32:35]\n"
        "\tv_bitop3_b32 v22, v22, v112, v252 bitop3:0xf6\n"
        "\tv_bitop3_b32 v22, v22, v113, v252 bitop3:0xf6\n"
        "\tv_mfma_f32_16x16x32_bf16 v[36:39], a[172:175], v[96:99], v[36:39]\n"
        "\tv_bitop3_b32 v22, v22, v114, v252 bitop3:0xf6\n"
        "\tv_bitop3_b32 v22, v22, v115, v252 bitop3:0xf6\n"
        "\tv_mfma_f32_16x16x32_bf16 v[24:27], a[176:179], v[100:103], v[24:27]\n"
        "\tv_bitop3_b32 v22, v22, v116, v252 bitop3:0xf6\n"
        "\tv_bitop3_b32 v22, v22, v117, v252 bitop3:0xf6\n"
        "\tv_mfma_f32_16x16x32_bf16 v[28:31], a[180:183], v[100:103], v[28:31]\n"
        "\tv_bitop3_b32 v22, v22, v118, v252 bitop3:0xf6\n"
        "\tv_bitop3_b32 v22, v22, v119, v252 bitop3:0xf6\n"
        "\tv_mfma_f32_16x16x32_bf16 v[32:35], a[184:187], v[100:103], v[32:35]\n"
        "\tv_and_b32 v22, s30, v22\n"
        "\tv_cmp_ne_u32 vcc, 0, v22\n"
        "\tv_mfma_f32_16x16x32_bf16 v[36:39], a[188:191], v[100:103], v[36:39]\n"
        "\ts_cbranch_vccnz LRh9\n"
        "LBh9:\n"
        "\tv_mfma_f32_16x16x32_bf16 v[40:43], a[128:131], v[104:107], v[40:43]\n"
        "\tbuffer_load_dwordx4 v[88:91], v1, s[8:11], s98 offen offset:1024 sc1\n"
        "\tv_and_b32 v9, 0x70, v1\n"
        "\tv_mfma_f32_16x16x32_bf16 v[44:47], a[132:135], v[104:107], v[44:47]\n"
        "\tds_write_b128 v14, v[24:27] offset:0\n"
        "\tv_lshrrev_b32 v21, 7, v1\n"
        "\tv_mfma_f32_16x16x32_bf16 v[48:51], a[136:139], v[104:107], v[48:51]\n"
        "\tds_write_b128 v14, v[28:31] offset:1024\n"
        "\tbuffer_load_dwordx4 v[92:95], v1, s[8:11], s99 offen offset:1024 sc1\n"
        "\tv_mfma_f32_16x16x32_bf16 v[52:55], a[140:143], v[104:107], v[52:55]\n"
        "\tds_write_b128 v14, v[32:35] offset:2048\n"
        "\tv_mul_u32_u24 v21, 144, v21\n"
        "\tv_mfma_f32_16x16x32_bf16 v[40:43], a[144:147], v[108:111], v[40:43]\n"
        "\tds_write_b128 v14, v[36:39] offset:3072\n"
        "\tbuffer_load_dwordx4 v[96:99], v1, s[8:11], s74 offen offset:1024 sc1\n"
        "\tv_mfma_f32_16x16x32_bf16 v[44:47], a[148:151], v[108:111], v[44:47]\n"
        "\tbuffer_load_dwordx4 v[100:103], v1, s[8:11], s76 offen offset:1024 sc1\n"
        "\ts_waitcnt vmcnt(10)\n"
        "\tv_mfma_f32_16x16x32_bf16 v[48:51], a[152:155], v[108:111], v[48:51]\n"
        "\tv_xor_b32 v22, v120, v252\n"
        "\tv_bitop3_b32 v22, v22, v121, v252 bitop3:0xf6\n"
        "\tv_mfma_f32_16x16x32_bf16 v[52:55], a[156:159], v[108:111], v[52:55]\n"
        "\tv_bitop3_b32 v22, v22, v122, v252 bitop3:0xf6\n"
        "\tv_bitop3_b32 v22, v22, v123, v252 bitop3:0xf6\n"
        "\tv_mfma_f32_16x16x32_bf16 v[40:43], a[160:163], v[112:115], v[40:43]\n"
        "\tv_bitop3_b32 v22, v22, v124, v252 bitop3:0xf6\n"
        "\tv_bitop3_b32 v22, v22, v125, v252 bitop3:0xf6\n"
        "\tv_mfma_f32_16x16x32_bf16 v[44:47], a[164:167], v[112:115], v[44:47]\n"
        "\tv_bitop3_b32 v22, v22, v126, v252 bitop3:0xf6\n"
        "\tv_bitop3_b32 v22, v22, v127, v252 bitop3:0xf6\n"
        "\tv_mfma_f32_16x16x32_bf16 v[48:51], a[168:171], v[112:115], v[48:51]\n"
        "\tv_bitop3_b32 v22, v22, v128, v252 bitop3:0xf6\n"
        "\tv_bitop3_b32 v22, v22, v129, v252 bitop3:0xf6\n"
        "\tv_mfma_f32_16x16x32_bf16 v[52:55], a[172:175], v[112:115], v[52:55]\n"
        "\tv_bitop3_b32 v22, v22, v130, v252 bitop3:0xf6\n"
        "\tv_bitop3_b32 v22, v22, v131, v252 bitop3:0xf6\n"
        "\tv_mfma_f32_16x16x32_bf16 v[40:43], a[176:179], v[116:119], v[40:43]\n"
        "\tv_bitop3_b32 v22, v22, v132, v252 bitop3:0xf6\n"
        "\tv_bitop3_b32 v22, v22, v133, v252 bitop3:0xf6\n"
        "\tv_mfma_f32_16x16x32_bf16 v[44:47], a[180:183], v[116:119], v[44:47]\n"
        "\tv_bitop3_b32 v22, v22, v134, v252 bitop3:0xf6\n"
        "\tv_bitop3_b32 v22, v22, v135, v252 bitop3:0xf6\n"
        "\tv_mfma_f32_16x16x32_bf16 v[48:51], a[184:187], v[116:119], v[48:51]\n"
        "\tv_and_b32 v22, s30, v22\n"
        "\tv_cmp_ne_u32 vcc, 0, v22\n"
        "\tv_mfma_f32_16x16x32_bf16 v[52:55], a[188:191], v[116:119], v[52:55]\n"
        "\tv_add3_u32 v9, v9, v21, s67\n"
        "\tv_bfe_u32 v21, v1, 4, 4\n"
        "\tv_mul_u32_u24 v21, 144, v21\n"
        "\tv_lshrrev_b32 v10, 8, v1\n"
        "\tv_lshl_add_u32 v21, v10, 3, v21\n"
        "\tv_add_u32 v10, s67, v21\n"
        "\ts_cbranch_vccnz LRh10\n"
        "LBh10:\n"
        "\tv_swap_b32 v122, v124\n"
        "\tv_swap_b32 v123, v125\n"
        "\tv_swap_b32 v130, v132\n"
        "\tv_swap_b32 v131, v133\n"
        "\tv_mfma_f32_16x16x32_bf16 v[56:59], a[192:195], v[124:127], v[56:59]\n"
        "\tbuffer_load_dwordx4 v[104:107], v1, s[8:11], s98 offen offset:2048 sc1\n"
        "\ts_lshl_b32 s55, s43, 2\n"
        "\tv_mfma_f32_16x16x32_bf16 v[60:63], a[196:199], v[124:127], v[60:63]\n"
        "\tds_write_b128 v15, v[40:43] offset:0\n"
        "\ts_add_u32 s55, s55, s77\n"
        "\tv_mfma_f32_16x16x32_bf16 v[64:67], a[200:203], v[124:127], v[64:67]\n"
        "\tds_write_b128 v15, v[44:47] offset:1024\n"
        "\tbuffer_load_dwordx4 v[108:111], v1, s[8:11], s99 offen offset:2048 sc1\n"
        "\tv_mfma_f32_16x16x32_bf16 v[68:71], a[204:207], v[124:127], v[68:71]\n"
        "\tds_write_b128 v15, v[48:51] offset:2048\n"
        "\ts_lshl_b32 s55, s55, 14\n"
        "\tv_mfma_f32_16x16x32_bf16 v[72:75], a[192:195], v[120:123], v[72:75]\n"
        "\tds_write_b128 v15, v[52:55] offset:3072\n"
        "\tbuffer_load_dwordx4 v[112:115], v1, s[8:11], s74 offen offset:2048 sc1\n"
        "\tv_mfma_f32_16x16x32_bf16 v[76:79], a[196:199], v[120:123], v[76:79]\n"
        "\tbuffer_load_dwordx4 v[116:119], v1, s[8:11], s76 offen offset:2048 sc1\n"
        "\ts_waitcnt vmcnt(9)\n"
        "\tv_mfma_f32_16x16x32_bf16 v[80:83], a[200:203], v[120:123], v[80:83]\n"
        "\tv_xor_b32 v22, v136, v252\n"
        "\tv_bitop3_b32 v22, v22, v137, v252 bitop3:0xf6\n"
        "\tv_mfma_f32_16x16x32_bf16 v[84:87], a[204:207], v[120:123], v[84:87]\n"
        "\tv_bitop3_b32 v22, v22, v138, v252 bitop3:0xf6\n"
        "\tv_bitop3_b32 v22, v22, v139, v252 bitop3:0xf6\n"
        "\tv_mfma_f32_16x16x32_bf16 v[56:59], a[208:211], v[132:135], v[56:59]\n"
        "\tv_bitop3_b32 v22, v22, v140, v252 bitop3:0xf6\n"
        "\tv_bitop3_b32 v22, v22, v141, v252 bitop3:0xf6\n"
        "\tv_mfma_f32_16x16x32_bf16 v[60:63], a[212:215], v[132:135], v[60:63]\n"
        "\tv_bitop3_b32 v22, v22, v142, v252 bitop3:0xf6\n"
        "\tv_bitop3_b32 v22, v22, v143, v252 bitop3:0xf6\n"
        "\tv_mfma_f32_16x16x32_bf16 v[64:67], a[216:219], v[132:135], v[64:67]\n"
        "\tv_bitop3_b32 v22, v22, v144, v252 bitop3:0xf6\n"
        "\tv_bitop3_b32 v22, v22, v145, v252 bitop3:0xf6\n"
        "\tv_mfma_f32_16x16x32_bf16 v[68:71], a[220:223], v[132:135], v[68:71]\n"
        "\tv_bitop3_b32 v22, v22, v146, v252 bitop3:0xf6\n"
        "\tv_bitop3_b32 v22, v22, v147, v252 bitop3:0xf6\n"
        "\tv_mfma_f32_16x16x32_bf16 v[72:75], a[208:211], v[128:131], v[72:75]\n"
        "\tv_bitop3_b32 v22, v22, v148, v252 bitop3:0xf6\n"
        "\tv_bitop3_b32 v22, v22, v149, v252 bitop3:0xf6\n"
        "\tv_mfma_f32_16x16x32_bf16 v[76:79], a[212:215], v[128:131], v[76:79]\n"
        "\tv_bitop3_b32 v22, v22, v150, v252 bitop3:0xf6\n"
        "\tv_bitop3_b32 v22, v22, v151, v252 bitop3:0xf6\n"
        "\tv_mfma_f32_16x16x32_bf16 v[80:83], a[216:219], v[128:131], v[80:83]\n"
        "\tv_and_b32 v22, s30, v22\n"
        "\tv_cmp_ne_u32 vcc, 0, v22\n"
        "\tv_mfma_f32_16x16x32_bf16 v[84:87], a[220:223], v[128:131], v[84:87]\n"
        "\ts_lshl_b32 s56, s44, 7\n"
        "\ts_add_u32 s55, s55, s56\n"
        "\ts_add_u32 s56, s55, 8192\n"
        "\ts_cbranch_vccnz LRh11\n"
        "LBh11:\n"
        "\tv_mfma_f32_16x16x32_bf16 v[56:59], a[128:131], v[136:139], v[56:59]\n"
        "\ts_waitcnt lgkmcnt(0)\n"
        "\ts_barrier\n"
        "\tv_mfma_f32_16x16x32_bf16 v[60:63], a[132:135], v[136:139], v[60:63]\n"
        "\ts_waitcnt vmcnt(8)\n"
        "\tds_read_b64_tr_b16 v[232:233], v13 offset:0\n"
        "\ts_waitcnt vmcnt(4)\n"
        "\tv_mfma_f32_16x16x32_bf16 v[64:67], a[136:139], v[136:139], v[64:67]\n"
        "\tds_read_b64_tr_b16 v[234:235], v13 offset:512\n"
        "\tv_xor_b32 v22, v88, v252\n"
        "\tv_mfma_f32_16x16x32_bf16 v[68:71], a[140:143], v[136:139], v[68:71]\n"
        "\tds_read_b64_tr_b16 v[236:237], v13 offset:1024\n"
        "\tv_bitop3_b32 v22, v22, v89, v252 bitop3:0xf6\n"
        "\tv_mfma_f32_16x16x32_bf16 v[56:59], a[144:147], v[140:143], v[56:59]\n"
        "\tds_read_b64_tr_b16 v[238:239], v13 offset:1536\n"
        "\tv_bitop3_b32 v22, v22, v90, v252 bitop3:0xf6\n"
        "\tv_mfma_f32_16x16x32_bf16 v[60:63], a[148:151], v[140:143], v[60:63]\n"
        "\tds_read_b128 v[184:187], v17 offset:0\n"
        "\tv_bitop3_b32 v22, v22, v91, v252 bitop3:0xf6\n"
        "\tv_mfma_f32_16x16x32_bf16 v[64:67], a[152:155], v[140:143], v[64:67]\n"
        "\tds_read_b128 v[200:203], v17 offset:4096\n"
        "\tv_bitop3_b32 v22, v22, v92, v252 bitop3:0xf6\n"
        "\tv_mfma_f32_16x16x32_bf16 v[68:71], a[156:159], v[140:143], v[68:71]\n"
        "\tds_read_b128 v[188:191], v17 offset:1024\n"
        "\tv_bitop3_b32 v22, v22, v93, v252 bitop3:0xf6\n"
        "\tv_mfma_f32_16x16x32_bf16 v[56:59], a[160:163], v[144:147], v[56:59]\n"
        "\tds_read_b128 v[204:207], v17 offset:5120\n"
        "\tv_bitop3_b32 v22, v22, v94, v252 bitop3:0xf6\n"
        "\tv_mfma_f32_16x16x32_bf16 v[60:63], a[164:167], v[144:147], v[60:63]\n"
        "\tds_read_b128 v[192:195], v17 offset:2048\n"
        "\tv_bitop3_b32 v22, v22, v95, v252 bitop3:0xf6\n"
        "\tv_mfma_f32_16x16x32_bf16 v[64:67], a[168:171], v[144:147], v[64:67]\n"
        "\tds_read_b128 v[208:211], v17 offset:6144\n"
        "\tv_bitop3_b32 v22, v22, v96, v252 bitop3:0xf6\n"
        "\tv_mfma_f32_16x16x32_bf16 v[68:71], a[172:175], v[144:147], v[68:71]\n"
        "\tds_read_b128 v[196:199], v17 offset:3072\n"
        "\tv_bitop3_b32 v22, v22, v97, v252 bitop3:0xf6\n"
        "\tv_mfma_f32_16x16x32_bf16 v[56:59], a[176:179], v[148:151], v[56:59]\n"
        "\tds_read_b128 v[212:215], v17 offset:7168\n"
        "\tv_bitop3_b32 v22, v22, v98, v252 bitop3:0xf6\n"
        "\tv_mfma_f32_16x16x32_bf16 v[60:63], a[180:183], v[148:151], v[60:63]\n"
        "\tv_bitop3_b32 v22, v22, v99, v252 bitop3:0xf6\n"
        "\tv_bitop3_b32 v22, v22, v100, v252 bitop3:0xf6\n"
        "\tv_mfma_f32_16x16x32_bf16 v[64:67], a[184:187], v[148:151], v[64:67]\n"
        "\tv_bitop3_b32 v22, v22, v101, v252 bitop3:0xf6\n"
        "\tv_bitop3_b32 v22, v22, v102, v252 bitop3:0xf6\n"
        "\tv_mfma_f32_16x16x32_bf16 v[68:71], a[188:191], v[148:151], v[68:71]\n"
        "\tv_bitop3_b32 v22, v22, v103, v252 bitop3:0xf6\n"
        "\tv_and_b32 v22, s30, v22\n"
        "\tv_cmp_ne_u32 vcc, 0, v22\n"
        "\ts_cbranch_vccnz LRh12\n"
        "LBh12:\n"
        "\tv_mfma_f32_16x16x32_bf16 v[72:75], a[16:19], v[88:91], v[72:75]\n"
        "\ts_waitcnt lgkmcnt(11)\n"
        "\tv_lshlrev_b32 v152, 16, v232\n"
        "\tv_and_b32 v153, s83, v232\n"
        "\tv_mfma_f32_16x16x32_bf16 v[76:79], a[20:23], v[88:91], v[76:79]\n"
        "\tds_write_b128 v16, v[56:59] offset:0\n"
        "\ts_waitcnt vmcnt(0)\n"
        "\tv_mfma_f32_16x16x32_bf16 v[80:83], a[24:27], v[88:91], v[80:83]\n"
        "\tds_write_b128 v16, v[60:63] offset:1024\n"
        "\tv_xor_b32 v22, v104, v252\n"
        "\tv_mfma_f32_16x16x32_bf16 v[84:87], a[28:31], v[88:91], v[84:87]\n"
        "\tds_write_b128 v16, v[64:67] offset:2048\n"
        "\tv_bitop3_b32 v22, v22, v105, v252 bitop3:0xf6\n"
        "\tv_mfma_f32_16x16x32_bf16 v[72:75], a[48:51], v[92:95], v[72:75]\n"
        "\tds_write_b128 v16, v[68:71] offset:3072\n"
        "\tv_bitop3_b32 v22, v22, v106, v252 bitop3:0xf6\n"
        "\tv_mfma_f32_16x16x32_bf16 v[76:79], a[52:55], v[92:95], v[76:79]\n"
        "\tv_bitop3_b32 v22, v22, v107, v252 bitop3:0xf6\n"
        "\tv_bitop3_b32 v22, v22, v108, v252 bitop3:0xf6\n"
        "\tv_mfma_f32_16x16x32_bf16 v[80:83], a[56:59], v[92:95], v[80:83]\n"
        "\tv_bitop3_b32 v22, v22, v109, v252 bitop3:0xf6\n"
        "\tv_bitop3_b32 v22, v22, v110, v252 bitop3:0xf6\n"
        "\tv_mfma_f32_16x16x32_bf16 v[84:87], a[60:63], v[92:95], v[84:87]\n"
        "\tv_bitop3_b32 v22, v22, v111, v252 bitop3:0xf6\n"
        "\tv_bitop3_b32 v22, v22, v112, v252 bitop3:0xf6\n"
        "\tv_mfma_f32_16x16x32_bf16 v[72:75], a[80:83], v[96:99], v[72:75]\n"
        "\ts_waitcnt lgkmcnt(0)\n"
        "\ts_barrier\n"
        "\tv_mfma_f32_16x16x32_bf16 v[76:79], a[84:87], v[96:99], v[76:79]\n"
        "\tds_read_b128 v[216:219], v17 offset:8192\n"
        "\tv_bitop3_b32 v22, v22, v113, v252 bitop3:0xf6\n"
        "\tv_mfma_f32_16x16x32_bf16 v[80:83], a[88:91], v[96:99], v[80:83]\n"
        "\tds_read_b128 v[220:223], v17 offset:9216\n"
        "\tv_bitop3_b32 v22, v22, v114, v252 bitop3:0xf6\n"
        "\tv_mfma_f32_16x16x32_bf16 v[84:87], a[92:95], v[96:99], v[84:87]\n"
        "\tds_read_b128 v[224:227], v17 offset:10240\n"
        "\tv_bitop3_b32 v22, v22, v115, v252 bitop3:0xf6\n"
        "\tv_mfma_f32_16x16x32_bf16 v[72:75], a[112:115], v[100:103], v[72:75]\n"
        "\tds_read_b128 v[228:231], v17 offset:11264\n"
        "\tv_bitop3_b32 v22, v22, v116, v252 bitop3:0xf6\n"
        "\tv_mfma_f32_16x16x32_bf16 v[76:79], a[116:119], v[100:103], v[76:79]\n"
        "\tv_bitop3_b32 v22, v22, v117, v252 bitop3:0xf6\n"
        "\tv_bitop3_b32 v22, v22, v118, v252 bitop3:0xf6\n"
        "\tv_mfma_f32_16x16x32_bf16 v[80:83], a[120:123], v[100:103], v[80:83]\n"
        "\tv_bitop3_b32 v22, v22, v119, v252 bitop3:0xf6\n"
        "\tv_and_b32 v22, s30, v22\n"
        "\tv_mfma_f32_16x16x32_bf16 v[84:87], a[124:127], v[100:103], v[84:87]\n"
        "\tv_cmp_ne_u32 vcc, 0, v22\n"
        "\tv_lshlrev_b32 v154, 16, v233\n"
        "\ts_cbranch_vccnz LRh13\n"
        "LBh13:\n"
        "\tv_mfma_f32_16x16x32_bf16 v[72:75], a[128:131], v[104:107], v[72:75]\n"
        "\tv_and_b32 v155, s83, v233\n"
        "\tv_lshlrev_b32 v156, 16, v234\n"
        "\tv_mfma_f32_16x16x32_bf16 v[76:79], a[132:135], v[104:107], v[76:79]\n"
        "\tv_and_b32 v157, s83, v234\n"
        "\tv_lshlrev_b32 v158, 16, v235\n"
        "\tv_mfma_f32_16x16x32_bf16 v[80:83], a[136:139], v[104:107], v[80:83]\n"
        "\tv_and_b32 v159, s83, v235\n"
        "\tv_lshlrev_b32 v160, 16, v236\n"
        "\tv_mfma_f32_16x16x32_bf16 v[84:87], a[140:143], v[104:107], v[84:87]\n"
        "\tv_and_b32 v161, s83, v236\n"
        "\tv_lshlrev_b32 v162, 16, v237\n"
        "\tv_mfma_f32_16x16x32_bf16 v[72:75], a[144:147], v[108:111], v[72:75]\n"
        "\tv_and_b32 v163, s83, v237\n"
        "\tv_lshlrev_b32 v164, 16, v238\n"
        "\tv_mfma_f32_16x16x32_bf16 v[76:79], a[148:151], v[108:111], v[76:79]\n"
        "\tv_and_b32 v165, s83, v238\n"
        "\tv_lshlrev_b32 v166, 16, v239\n"
        "\tv_mfma_f32_16x16x32_bf16 v[80:83], a[152:155], v[108:111], v[80:83]\n"
        "\tv_and_b32 v167, s83, v239\n"
        "\tv_add_f32 v184, v184, v200\n"
        "\tv_mfma_f32_16x16x32_bf16 v[84:87], a[156:159], v[108:111], v[84:87]\n"
        "\tv_add_f32 v185, v185, v201\n"
        "\tv_add_f32 v186, v186, v202\n"
        "\tv_mfma_f32_16x16x32_bf16 v[72:75], a[160:163], v[112:115], v[72:75]\n"
        "\tv_add_f32 v187, v187, v203\n"
        "\tv_add_f32 v188, v188, v204\n"
        "\tv_mfma_f32_16x16x32_bf16 v[76:79], a[164:167], v[112:115], v[76:79]\n"
        "\tv_add_f32 v189, v189, v205\n"
        "\tv_add_f32 v190, v190, v206\n"
        "\tv_mfma_f32_16x16x32_bf16 v[80:83], a[168:171], v[112:115], v[80:83]\n"
        "\tv_add_f32 v191, v191, v207\n"
        "\tv_add_f32 v192, v192, v208\n"
        "\tv_mfma_f32_16x16x32_bf16 v[84:87], a[172:175], v[112:115], v[84:87]\n"
        "\tv_add_f32 v193, v193, v209\n"
        "\tv_add_f32 v194, v194, v210\n"
        "\tv_mfma_f32_16x16x32_bf16 v[72:75], a[176:179], v[116:119], v[72:75]\n"
        "\tv_add_f32 v195, v195, v211\n"
        "\tv_add_f32 v196, v196, v212\n"
        "\tv_mfma_f32_16x16x32_bf16 v[76:79], a[180:183], v[116:119], v[76:79]\n"
        "\tv_add_f32 v197, v197, v213\n"
        "\tv_add_f32 v198, v198, v214\n"
        "\tv_mfma_f32_16x16x32_bf16 v[80:83], a[184:187], v[116:119], v[80:83]\n"
        "\tv_add_f32 v199, v199, v215\n"
        "\ts_waitcnt lgkmcnt(3)\n"
        "\tv_add_f32 v184, v184, v216\n"
        "\tv_mfma_f32_16x16x32_bf16 v[84:87], a[188:191], v[116:119], v[84:87]\n"
        "\tv_add_f32 v185, v185, v217\n"
        "\tv_add_f32 v186, v186, v218\n"
        "\tv_add_f32 v187, v187, v219\n"
        "\ts_waitcnt lgkmcnt(2)\n"
        "\tv_add_f32 v188, v188, v220\n"
        "\tv_add_f32 v189, v189, v221\n"
        "\tv_add_f32 v190, v190, v222\n"
        "\tv_add_f32 v191, v191, v223\n"
        "\ts_waitcnt lgkmcnt(1)\n"
        "\tv_add_f32 v192, v192, v224\n"
        "\tv_add_f32 v193, v193, v225\n"
        "\tv_add_f32 v194, v194, v226\n"
        "\tv_add_f32 v195, v195, v227\n"
        "\ts_waitcnt lgkmcnt(0)\n"
        "\tv_add_f32 v196, v196, v228\n"
        "\tv_add_f32 v197, v197, v229\n"
        "\tv_add_f32 v198, v198, v230\n"
        "\tv_add_f32 v199, v199, v231\n"
        "\tv_pk_add_f32 v[72:73], v[72:73], v[184:185]\n"
        "\tv_pk_fma_f32 v[72:73], v[72:73], v[18:19], v[152:153] op_sel:[0,1,0] op_sel_hi:[1,1,1]\n"
        "\tv_cvt_pk_bf16_f32 v240, v72, v73\n"
        "\tv_pk_add_f32 v[74:75], v[74:75], v[186:187]\n"
        "\tv_pk_fma_f32 v[74:75], v[74:75], v[18:19], v[154:155] op_sel:[0,1,0] op_sel_hi:[1,1,1]\n"
        "\tv_cvt_pk_bf16_f32 v241, v74, v75\n"
        "\tds_write_b64 v10, v[240:241] offset:0\n"
        "\tv_pk_add_f32 v[76:77], v[76:77], v[188:189]\n"
        "\tv_pk_fma_f32 v[76:77], v[76:77], v[18:19], v[156:157] op_sel:[0,1,0] op_sel_hi:[1,1,1]\n"
        "\tv_cvt_pk_bf16_f32 v242, v76, v77\n"
        "\tv_pk_add_f32 v[78:79], v[78:79], v[190:191]\n"
        "\tv_pk_fma_f32 v[78:79], v[78:79], v[18:19], v[158:159] op_sel:[0,1,0] op_sel_hi:[1,1,1]\n"
        "\tv_cvt_pk_bf16_f32 v243, v78, v79\n"
        "\tds_write_b64 v10, v[242:243] offset:32\n"
        "\tv_pk_add_f32 v[80:81], v[80:81], v[192:193]\n"
        "\tv_pk_fma_f32 v[80:81], v[80:81], v[18:19], v[160:161] op_sel:[0,1,0] op_sel_hi:[1,1,1]\n"
        "\tv_cvt_pk_bf16_f32 v244, v80, v81\n"
        "\tv_pk_add_f32 v[82:83], v[82:83], v[194:195]\n"
        "\tv_pk_fma_f32 v[82:83], v[82:83], v[18:19], v[162:163] op_sel:[0,1,0] op_sel_hi:[1,1,1]\n"
        "\tv_cvt_pk_bf16_f32 v245, v82, v83\n"
        "\tds_write_b64 v10, v[244:245] offset:64\n"
        "\tv_pk_add_f32 v[84:85], v[84:85], v[196:197]\n"
        "\tv_pk_fma_f32 v[84:85], v[84:85], v[18:19], v[164:165] op_sel:[0,1,0] op_sel_hi:[1,1,1]\n"
        "\tv_cvt_pk_bf16_f32 v246, v84, v85\n"
        "\tv_pk_add_f32 v[86:87], v[86:87], v[198:199]\n"
        "\tv_pk_fma_f32 v[86:87], v[86:87], v[18:19], v[166:167] op_sel:[0,1,0] op_sel_hi:[1,1,1]\n"
        "\tv_cvt_pk_bf16_f32 v247, v86, v87\n"
        "\tds_write_b64 v10, v[246:247] offset:96\n"
        "\tds_read_b128 v[232:235], v9 offset:0\n"
        "\tds_read_b128 v[236:239], v9 offset:1152\n"
        "\ts_waitcnt lgkmcnt(1)\n"
        "\tbuffer_store_dwordx4 v[232:235], v18, s[20:23], s55 offen sc1\n"
        "\ts_waitcnt lgkmcnt(0)\n"
        "\tbuffer_store_dwordx4 v[236:239], v18, s[20:23], s56 offen sc1\n"
        "\ts_endpgm\n"
        "LRx:\n"
        "\ts_waitcnt vmcnt(0) lgkmcnt(0)\n"
        "\ts_nop 7\n"
        "\ts_sleep 1\n"
        "\tbuffer_load_dwordx4 v[156:159], v24, s[8:11], s97 offen sc1\n"
        "\tbuffer_load_dwordx4 v[160:163], v24, s[8:11], s98 offen sc1\n"
        "\tbuffer_load_dwordx4 v[164:167], v24, s[8:11], s99 offen sc1\n"
        "\tbuffer_load_dwordx4 v[168:171], v24, s[8:11], s63 offen sc1\n"
        "\tbuffer_load_dwordx4 v[172:175], v24, s[8:11], s68 offen sc1\n"
        "\tbuffer_load_dwordx4 v[176:179], v24, s[8:11], s65 offen sc1\n"
        "\tbuffer_load_dwordx2 v[180:181], v228, s[8:11], s69 offen sc1\n"
        "\ts_waitcnt vmcnt(0)\n"
        "\tv_mad_u64_u32 v[182:183], s[94:95], v172, v220, 0\n"
        "\tv_mad_u64_u32 v[182:183], s[94:95], v173, v221, v[182:183]\n"
        "\tv_mad_u64_u32 v[182:183], s[94:95], v174, v222, v[182:183]\n"
        "\tv_mad_u64_u32 v[182:183], s[94:95], v175, v223, v[182:183]\n"
        "\tv_mad_u64_u32 v[182:183], s[94:95], v176, v224, v[182:183]\n"
        "\tv_mad_u64_u32 v[182:183], s[94:95], v177, v225, v[182:183]\n"
        "\tv_mad_u64_u32 v[182:183], s[94:95], v178, v226, v[182:183]\n"
        "\tv_mad_u64_u32 v[182:183], s[94:95], v179, v227, v[182:183]\n"
        "\ts_nop 1\n"
        "\tv_mov_b32_dpp v184, v182 quad_perm:[1,0,3,2] row_mask:0xf bank_mask:0xf\n"
        "\tv_mov_b32_dpp v185, v183 quad_perm:[1,0,3,2] row_mask:0xf bank_mask:0xf\n"
        "\tv_lshl_add_u64 v[182:183], v[182:183], 0, v[184:185]\n"
        "\ts_nop 1\n"
        "\tv_mov_b32_dpp v184, v182 quad_perm:[2,3,0,1] row_mask:0xf bank_mask:0xf\n"
        "\tv_mov_b32_dpp v185, v183 quad_perm:[2,3,0,1] row_mask:0xf bank_mask:0xf\n"
        "\tv_lshl_add_u64 v[182:183], v[182:183], 0, v[184:185]\n"
        "\tv_lshl_add_u64 v[182:183], v[182:183], 0, v[20:21]\n"
        "\tv_cmp_ne_u64 vcc, v[182:183], v[180:181]\n"
        "\tv_mad_u64_u32 v[184:185], s[94:95], v156, s86, v[20:21]\n"
        "\tv_mad_u64_u32 v[184:185], s[94:95], v157, s87, v[184:185]\n"
        "\tv_cmp_ne_u64 s[58:59], v[184:185], v[158:159]\n"
        "\ts_or_b64 vcc, vcc, s[58:59]\n"
        "\tv_mad_u64_u32 v[184:185], s[94:95], v160, s86, v[20:21]\n"
        "\tv_mad_u64_u32 v[184:185], s[94:95], v161, s87, v[184:185]\n"
        "\tv_cmp_ne_u64 s[58:59], v[184:185], v[162:163]\n"
        "\ts_or_b64 vcc, vcc, s[58:59]\n"
        "\tv_mad_u64_u32 v[184:185], s[94:95], v164, s86, v[20:21]\n"
        "\tv_mad_u64_u32 v[184:185], s[94:95], v165, s87, v[184:185]\n"
        "\tv_cmp_ne_u64 s[58:59], v[184:185], v[166:167]\n"
        "\ts_or_b64 vcc, vcc, s[58:59]\n"
        "\tv_mad_u64_u32 v[184:185], s[94:95], v168, s86, v[20:21]\n"
        "\tv_mad_u64_u32 v[184:185], s[94:95], v169, s87, v[184:185]\n"
        "\tv_cmp_ne_u64 s[58:59], v[184:185], v[170:171]\n"
        "\ts_or_b64 vcc, vcc, s[58:59]\n"
        "\ts_nop 4\n"
        "\ts_cbranch_vccz LBx\n"
        "\ts_cmp_eq_u32 s54, 0\n"
        "\ts_cbranch_scc1 LBx\n"
        "\ts_sub_u32 s54, s54, 1\n"
        "\ts_branch LRx\n"
        "LRfa:\n"
        "\ts_waitcnt vmcnt(0) lgkmcnt(0)\n"
        "\ts_nop 7\n"
        "\ts_sleep 1\n"
        "\tbuffer_load_dwordx2 v[228:229], v230, s[8:11], s60 offen sc1\n"
        "\ts_waitcnt vmcnt(0)\n"
        "\tv_cmp_ne_u64 vcc, v[228:229], v[20:21]\n"
        "\ts_nop 4\n"
        "\ts_cbranch_vccz LBfa\n"
        "\ts_cmp_eq_u32 s54, 0\n"
        "\ts_cbranch_scc1 LBfa\n"
        "\ts_sub_u32 s54, s54, 1\n"
        "\ts_branch LRfa\n"
        "LRf:\n"
        "\ts_waitcnt vmcnt(0) lgkmcnt(0)\n"
        "\ts_nop 7\n"
        "\ts_sleep 1\n"
        "\ts_movk_i32 s35, 24576\n"
        "\tbuffer_load_dwordx2 v[232:233], v230, s[8:11], s35 offen sc1\n"
        "\ts_waitcnt vmcnt(0)\n"
        "\tv_xor_b32 v234, v232, v20\n"
        "\tv_xor_b32 v235, v233, v21\n"
        "\tv_and_or_b32 v234, v234, s83, v235\n"
        "\tv_cmp_ne_u32 vcc, 0, v234\n"
        "\ts_nop 4\n"
        "\ts_cbranch_vccz LBf\n"
        "\ts_cmp_eq_u32 s54, 0\n"
        "\ts_cbranch_scc1 LBf\n"
        "\ts_sub_u32 s54, s54, 1\n"
        "\ts_branch LRf\n"
        "LRa0:\n"
        "\ts_waitcnt vmcnt(0) lgkmcnt(0)\n"
        "\ts_nop 7\n"
        "\ts_sleep 1\n"
        "\ts_add_u32 m0, s67, 0\n"
        "\ts_add_u32 s96, s73, 0\n"
        "\tbuffer_load_dwordx4 v2, s[8:11], s96 offen sc1 lds\n"
        "\tbuffer_load_dwordx4 v2, s[8:11], s96 offen offset:1024 sc1 lds\n"
        "\ts_waitcnt vmcnt(0)\n"
        "\tds_read_b64_tr_b16 v[24:25], v3 offset:0\n"
        "\tds_read_b64_tr_b16 v[26:27], v3 offset:1024\n"
        "\tds_read_b64_tr_b16 v[28:29], v4 offset:0\n"
        "\tds_read_b64_tr_b16 v[30:31], v4 offset:1024\n"
        "\ts_waitcnt lgkmcnt(2)\n"
        "\tv_mfma_f32_16x16x32_bf16 v[184:187], a[224:227], v[24:27], 0\n"
        "\ts_waitcnt lgkmcnt(0)\n"
        "\tv_mfma_f32_16x16x32_bf16 v[188:191], a[224:227], v[28:31], 0\n"
        "\ts_nop 7\n"
        "\tv_cmp_u_f32 vcc, v184, v188\n"
        "\ts_nop 4\n"
        "\ts_cbranch_vccz LTa0\n"
        "\ts_cmp_eq_u32 s54, 0\n"
        "\ts_cbranch_scc1 LTa0\n"
        "\ts_sub_u32 s54, s54, 1\n"
        "\ts_branch LRa0\n"
        "LTa0:\n"
        "\ts_add_u32 m0, s67, 0\n"
        "\ts_add_u32 s96, s73, 12288\n"
        "\tbuffer_load_dwordx4 v2, s[8:11], s96 offen sc1 lds\n"
        "\tbuffer_load_dwordx4 v2, s[8:11], s96 offen offset:1024 sc1 lds\n"
        "\ts_waitcnt vmcnt(0) lgkmcnt(0)\n"
        "\ts_nop 7\n"
        "\ts_branch LBa0\n"
        "LRa1:\n"
        "\ts_waitcnt vmcnt(0) lgkmcnt(0)\n"
        "\ts_nop 7\n"
        "\ts_sleep 1\n"
        "\ts_add_u32 m0, s67, 2048\n"
        "\ts_add_u32 s96, s73, 2048\n"
        "\tbuffer_load_dwordx4 v2, s[8:11], s96 offen sc1 lds\n"
        "\tbuffer_load_dwordx4 v2, s[8:11], s96 offen offset:1024 sc1 lds\n"
        "\ts_waitcnt vmcnt(0)\n"
        "\tds_read_b64_tr_b16 v[32:33], v3 offset:2048\n"
        "\tds_read_b64_tr_b16 v[34:35], v3 offset:3072\n"
        "\tds_read_b64_tr_b16 v[36:37], v4 offset:2048\n"
        "\tds_read_b64_tr_b16 v[38:39], v4 offset:3072\n"
        "\ts_waitcnt lgkmcnt(2)\n"
        "\tv_mfma_f32_16x16x32_bf16 v[192:195], a[224:227], v[32:35], v[184:187]\n"
        "\ts_waitcnt lgkmcnt(0)\n"
        "\tv_mfma_f32_16x16x32_bf16 v[196:199], a[224:227], v[36:39], v[188:191]\n"
        "\ts_nop 7\n"
        "\tv_cmp_u_f32 vcc, v192, v196\n"
        "\ts_nop 4\n"
        "\ts_cbranch_vccz LTa1\n"
        "\ts_cmp_eq_u32 s54, 0\n"
        "\ts_cbranch_scc1 LTa1\n"
        "\ts_sub_u32 s54, s54, 1\n"
        "\ts_branch LRa1\n"
        "LTa1:\n"
        "\ts_add_u32 m0, s67, 2048\n"
        "\ts_add_u32 s96, s73, 14336\n"
        "\tbuffer_load_dwordx4 v2, s[8:11], s96 offen sc1 lds\n"
        "\tbuffer_load_dwordx4 v2, s[8:11], s96 offen offset:1024 sc1 lds\n"
        "\ts_waitcnt vmcnt(0) lgkmcnt(0)\n"
        "\ts_nop 7\n"
        "\ts_branch LBa1\n"
        "LRa2:\n"
        "\ts_waitcnt vmcnt(0) lgkmcnt(0)\n"
        "\ts_nop 7\n"
        "\ts_sleep 1\n"
        "\ts_add_u32 m0, s67, 4096\n"
        "\ts_add_u32 s96, s73, 4096\n"
        "\tbuffer_load_dwordx4 v2, s[8:11], s96 offen sc1 lds\n"
        "\tbuffer_load_dwordx4 v2, s[8:11], s96 offen offset:1024 sc1 lds\n"
        "\ts_waitcnt vmcnt(0)\n"
        "\tds_read_b64_tr_b16 v[40:41], v3 offset:4096\n"
        "\tds_read_b64_tr_b16 v[42:43], v3 offset:5120\n"
        "\tds_read_b64_tr_b16 v[44:45], v4 offset:4096\n"
        "\tds_read_b64_tr_b16 v[46:47], v4 offset:5120\n"
        "\ts_waitcnt lgkmcnt(2)\n"
        "\tv_mfma_f32_16x16x32_bf16 v[184:187], a[224:227], v[40:43], v[192:195]\n"
        "\ts_waitcnt lgkmcnt(0)\n"
        "\tv_mfma_f32_16x16x32_bf16 v[188:191], a[224:227], v[44:47], v[196:199]\n"
        "\ts_nop 7\n"
        "\tv_cmp_u_f32 vcc, v184, v188\n"
        "\ts_nop 4\n"
        "\ts_cbranch_vccz LTa2\n"
        "\ts_cmp_eq_u32 s54, 0\n"
        "\ts_cbranch_scc1 LTa2\n"
        "\ts_sub_u32 s54, s54, 1\n"
        "\ts_branch LRa2\n"
        "LTa2:\n"
        "\ts_add_u32 m0, s67, 4096\n"
        "\ts_add_u32 s96, s73, 16384\n"
        "\tbuffer_load_dwordx4 v2, s[8:11], s96 offen sc1 lds\n"
        "\tbuffer_load_dwordx4 v2, s[8:11], s96 offen offset:1024 sc1 lds\n"
        "\ts_waitcnt vmcnt(0) lgkmcnt(0)\n"
        "\ts_nop 7\n"
        "\ts_branch LBa2\n"
        "LRa3:\n"
        "\ts_waitcnt vmcnt(0) lgkmcnt(0)\n"
        "\ts_nop 7\n"
        "\ts_sleep 1\n"
        "\ts_add_u32 m0, s67, 6144\n"
        "\ts_add_u32 s96, s73, 6144\n"
        "\tbuffer_load_dwordx4 v2, s[8:11], s96 offen sc1 lds\n"
        "\tbuffer_load_dwordx4 v2, s[8:11], s96 offen offset:1024 sc1 lds\n"
        "\ts_waitcnt vmcnt(0)\n"
        "\tds_read_b64_tr_b16 v[48:49], v3 offset:6144\n"
        "\tds_read_b64_tr_b16 v[50:51], v3 offset:7168\n"
        "\tds_read_b64_tr_b16 v[52:53], v4 offset:6144\n"
        "\tds_read_b64_tr_b16 v[54:55], v4 offset:7168\n"
        "\ts_waitcnt lgkmcnt(2)\n"
        "\tv_mfma_f32_16x16x32_bf16 v[192:195], a[224:227], v[48:51], v[184:187]\n"
        "\ts_waitcnt lgkmcnt(0)\n"
        "\tv_mfma_f32_16x16x32_bf16 v[196:199], a[224:227], v[52:55], v[188:191]\n"
        "\ts_nop 7\n"
        "\tv_cmp_u_f32 vcc, v192, v196\n"
        "\ts_nop 4\n"
        "\ts_cbranch_vccz LTa3\n"
        "\ts_cmp_eq_u32 s54, 0\n"
        "\ts_cbranch_scc1 LTa3\n"
        "\ts_sub_u32 s54, s54, 1\n"
        "\ts_branch LRa3\n"
        "LTa3:\n"
        "\ts_add_u32 m0, s67, 6144\n"
        "\ts_add_u32 s96, s73, 18432\n"
        "\tbuffer_load_dwordx4 v2, s[8:11], s96 offen sc1 lds\n"
        "\tbuffer_load_dwordx4 v2, s[8:11], s96 offen offset:1024 sc1 lds\n"
        "\ts_waitcnt vmcnt(0) lgkmcnt(0)\n"
        "\ts_nop 7\n"
        "\ts_branch LBa3\n"
        "LRa4:\n"
        "\ts_waitcnt vmcnt(0) lgkmcnt(0)\n"
        "\ts_nop 7\n"
        "\ts_sleep 1\n"
        "\ts_add_u32 m0, s67, 8192\n"
        "\ts_add_u32 s96, s73, 8192\n"
        "\tbuffer_load_dwordx4 v2, s[8:11], s96 offen sc1 lds\n"
        "\tbuffer_load_dwordx4 v2, s[8:11], s96 offen offset:1024 sc1 lds\n"
        "\ts_waitcnt vmcnt(0)\n"
        "\tds_read_b64_tr_b16 v[56:57], v3 offset:8192\n"
        "\tds_read_b64_tr_b16 v[58:59], v3 offset:9216\n"
        "\tds_read_b64_tr_b16 v[60:61], v4 offset:8192\n"
        "\tds_read_b64_tr_b16 v[62:63], v4 offset:9216\n"
        "\ts_waitcnt lgkmcnt(2)\n"
        "\tv_mfma_f32_16x16x32_bf16 v[184:187], a[224:227], v[56:59], v[192:195]\n"
        "\ts_waitcnt lgkmcnt(0)\n"
        "\tv_mfma_f32_16x16x32_bf16 v[188:191], a[224:227], v[60:63], v[196:199]\n"
        "\ts_nop 7\n"
        "\tv_cmp_u_f32 vcc, v184, v188\n"
        "\ts_nop 4\n"
        "\ts_cbranch_vccz LTa4\n"
        "\ts_cmp_eq_u32 s54, 0\n"
        "\ts_cbranch_scc1 LTa4\n"
        "\ts_sub_u32 s54, s54, 1\n"
        "\ts_branch LRa4\n"
        "LTa4:\n"
        "\ts_add_u32 m0, s67, 8192\n"
        "\ts_add_u32 s96, s73, 20480\n"
        "\tbuffer_load_dwordx4 v2, s[8:11], s96 offen sc1 lds\n"
        "\tbuffer_load_dwordx4 v2, s[8:11], s96 offen offset:1024 sc1 lds\n"
        "\ts_waitcnt vmcnt(0) lgkmcnt(0)\n"
        "\ts_nop 7\n"
        "\ts_branch LBa4\n"
        "LRa5:\n"
        "\ts_waitcnt vmcnt(0) lgkmcnt(0)\n"
        "\ts_nop 7\n"
        "\ts_sleep 1\n"
        "\ts_add_u32 m0, s67, 10240\n"
        "\ts_add_u32 s96, s73, 10240\n"
        "\tbuffer_load_dwordx4 v2, s[8:11], s96 offen sc1 lds\n"
        "\tbuffer_load_dwordx4 v2, s[8:11], s96 offen offset:1024 sc1 lds\n"
        "\ts_waitcnt vmcnt(0)\n"
        "\tds_read_b64_tr_b16 v[64:65], v3 offset:10240\n"
        "\tds_read_b64_tr_b16 v[66:67], v3 offset:11264\n"
        "\tds_read_b64_tr_b16 v[68:69], v4 offset:10240\n"
        "\tds_read_b64_tr_b16 v[70:71], v4 offset:11264\n"
        "\ts_waitcnt lgkmcnt(2)\n"
        "\tv_mfma_f32_16x16x32_bf16 v[192:195], a[224:227], v[64:67], v[184:187]\n"
        "\ts_waitcnt lgkmcnt(0)\n"
        "\tv_mfma_f32_16x16x32_bf16 v[196:199], a[224:227], v[68:71], v[188:191]\n"
        "\ts_nop 7\n"
        "\tv_cmp_u_f32 vcc, v192, v196\n"
        "\ts_nop 4\n"
        "\ts_cbranch_vccz LTa5\n"
        "\ts_cmp_eq_u32 s54, 0\n"
        "\ts_cbranch_scc1 LTa5\n"
        "\ts_sub_u32 s54, s54, 1\n"
        "\ts_branch LRa5\n"
        "LTa5:\n"
        "\ts_add_u32 m0, s67, 10240\n"
        "\ts_add_u32 s96, s73, 22528\n"
        "\tbuffer_load_dwordx4 v2, s[8:11], s96 offen sc1 lds\n"
        "\tbuffer_load_dwordx4 v2, s[8:11], s96 offen offset:1024 sc1 lds\n"
        "\ts_waitcnt vmcnt(0) lgkmcnt(0)\n"
        "\ts_nop 7\n"
        "\ts_branch LBa5\n"
        "LRa6:\n"
        "\ts_waitcnt vmcnt(0) lgkmcnt(0)\n"
        "\ts_nop 7\n"
        "\ts_sleep 1\n"
        "\ts_add_u32 m0, s67, 0\n"
        "\ts_add_u32 s96, s73, 12288\n"
        "\tbuffer_load_dwordx4 v2, s[8:11], s96 offen sc1 lds\n"
        "\tbuffer_load_dwordx4 v2, s[8:11], s96 offen offset:1024 sc1 lds\n"
        "\ts_waitcnt vmcnt(0)\n"
        "\tds_read_b64_tr_b16 v[72:73], v3 offset:0\n"
        "\tds_read_b64_tr_b16 v[74:75], v3 offset:1024\n"
        "\tds_read_b64_tr_b16 v[76:77], v4 offset:0\n"
        "\tds_read_b64_tr_b16 v[78:79], v4 offset:1024\n"
        "\ts_waitcnt lgkmcnt(2)\n"
        "\tv_mfma_f32_16x16x32_bf16 v[184:187], a[224:227], v[72:75], v[192:195]\n"
        "\ts_waitcnt lgkmcnt(0)\n"
        "\tv_mfma_f32_16x16x32_bf16 v[188:191], a[224:227], v[76:79], v[196:199]\n"
        "\ts_nop 7\n"
        "\tv_cmp_u_f32 vcc, v184, v188\n"
        "\ts_nop 4\n"
        "\ts_cbranch_vccz LTa6\n"
        "\ts_cmp_eq_u32 s54, 0\n"
        "\ts_cbranch_scc1 LTa6\n"
        "\ts_sub_u32 s54, s54, 1\n"
        "\ts_branch LRa6\n"
        "LTa6:\n"
        "\ts_add_u32 m0, s67, 0\n"
        "\ts_add_u32 s96, s73, 24576\n"
        "\tbuffer_load_dwordx4 v2, s[8:11], s96 offen sc1 lds\n"
        "\tbuffer_load_dwordx4 v2, s[8:11], s96 offen offset:1024 sc1 lds\n"
        "\ts_waitcnt vmcnt(0) lgkmcnt(0)\n"
        "\ts_nop 7\n"
        "\ts_branch LBa6\n"
        "LRa7:\n"
        "\ts_waitcnt vmcnt(0) lgkmcnt(0)\n"
        "\ts_nop 7\n"
        "\ts_sleep 1\n"
        "\ts_add_u32 m0, s67, 2048\n"
        "\ts_add_u32 s96, s73, 14336\n"
        "\tbuffer_load_dwordx4 v2, s[8:11], s96 offen sc1 lds\n"
        "\tbuffer_load_dwordx4 v2, s[8:11], s96 offen offset:1024 sc1 lds\n"
        "\ts_waitcnt vmcnt(0)\n"
        "\tds_read_b64_tr_b16 v[80:81], v3 offset:2048\n"
        "\tds_read_b64_tr_b16 v[82:83], v3 offset:3072\n"
        "\tds_read_b64_tr_b16 v[84:85], v4 offset:2048\n"
        "\tds_read_b64_tr_b16 v[86:87], v4 offset:3072\n"
        "\ts_waitcnt lgkmcnt(2)\n"
        "\tv_mfma_f32_16x16x32_bf16 v[192:195], a[224:227], v[80:83], v[184:187]\n"
        "\ts_waitcnt lgkmcnt(0)\n"
        "\tv_mfma_f32_16x16x32_bf16 v[196:199], a[224:227], v[84:87], v[188:191]\n"
        "\ts_nop 7\n"
        "\tv_cmp_u_f32 vcc, v192, v196\n"
        "\ts_nop 4\n"
        "\ts_cbranch_vccz LTa7\n"
        "\ts_cmp_eq_u32 s54, 0\n"
        "\ts_cbranch_scc1 LTa7\n"
        "\ts_sub_u32 s54, s54, 1\n"
        "\ts_branch LRa7\n"
        "LTa7:\n"
        "\ts_add_u32 m0, s67, 2048\n"
        "\ts_add_u32 s96, s73, 26624\n"
        "\tbuffer_load_dwordx4 v2, s[8:11], s96 offen sc1 lds\n"
        "\tbuffer_load_dwordx4 v2, s[8:11], s96 offen offset:1024 sc1 lds\n"
        "\ts_waitcnt vmcnt(0) lgkmcnt(0)\n"
        "\ts_nop 7\n"
        "\ts_branch LBa7\n"
        "LRa8:\n"
        "\ts_waitcnt vmcnt(0) lgkmcnt(0)\n"
        "\ts_nop 7\n"
        "\ts_sleep 1\n"
        "\ts_add_u32 m0, s67, 4096\n"
        "\ts_add_u32 s96, s73, 16384\n"
        "\tbuffer_load_dwordx4 v2, s[8:11], s96 offen sc1 lds\n"
        "\tbuffer_load_dwordx4 v2, s[8:11], s96 offen offset:1024 sc1 lds\n"
        "\ts_waitcnt vmcnt(0)\n"
        "\tds_read_b64_tr_b16 v[88:89], v3 offset:4096\n"
        "\tds_read_b64_tr_b16 v[90:91], v3 offset:5120\n"
        "\tds_read_b64_tr_b16 v[92:93], v4 offset:4096\n"
        "\tds_read_b64_tr_b16 v[94:95], v4 offset:5120\n"
        "\ts_waitcnt lgkmcnt(2)\n"
        "\tv_mfma_f32_16x16x32_bf16 v[184:187], a[224:227], v[88:91], v[192:195]\n"
        "\ts_waitcnt lgkmcnt(0)\n"
        "\tv_mfma_f32_16x16x32_bf16 v[188:191], a[224:227], v[92:95], v[196:199]\n"
        "\ts_nop 7\n"
        "\tv_cmp_u_f32 vcc, v184, v188\n"
        "\ts_nop 4\n"
        "\ts_cbranch_vccz LTa8\n"
        "\ts_cmp_eq_u32 s54, 0\n"
        "\ts_cbranch_scc1 LTa8\n"
        "\ts_sub_u32 s54, s54, 1\n"
        "\ts_branch LRa8\n"
        "LTa8:\n"
        "\ts_add_u32 m0, s67, 4096\n"
        "\ts_add_u32 s96, s73, 28672\n"
        "\tbuffer_load_dwordx4 v2, s[8:11], s96 offen sc1 lds\n"
        "\tbuffer_load_dwordx4 v2, s[8:11], s96 offen offset:1024 sc1 lds\n"
        "\ts_waitcnt vmcnt(0) lgkmcnt(0)\n"
        "\ts_nop 7\n"
        "\ts_branch LBa8\n"
        "LRa9:\n"
        "\ts_waitcnt vmcnt(0) lgkmcnt(0)\n"
        "\ts_nop 7\n"
        "\ts_sleep 1\n"
        "\ts_add_u32 m0, s67, 6144\n"
        "\ts_add_u32 s96, s73, 18432\n"
        "\tbuffer_load_dwordx4 v2, s[8:11], s96 offen sc1 lds\n"
        "\tbuffer_load_dwordx4 v2, s[8:11], s96 offen offset:1024 sc1 lds\n"
        "\ts_waitcnt vmcnt(0)\n"
        "\tds_read_b64_tr_b16 v[96:97], v3 offset:6144\n"
        "\tds_read_b64_tr_b16 v[98:99], v3 offset:7168\n"
        "\tds_read_b64_tr_b16 v[100:101], v4 offset:6144\n"
        "\tds_read_b64_tr_b16 v[102:103], v4 offset:7168\n"
        "\ts_waitcnt lgkmcnt(2)\n"
        "\tv_mfma_f32_16x16x32_bf16 v[192:195], a[224:227], v[96:99], v[184:187]\n"
        "\ts_waitcnt lgkmcnt(0)\n"
        "\tv_mfma_f32_16x16x32_bf16 v[196:199], a[224:227], v[100:103], v[188:191]\n"
        "\ts_nop 7\n"
        "\tv_cmp_u_f32 vcc, v192, v196\n"
        "\ts_nop 4\n"
        "\ts_cbranch_vccz LTa9\n"
        "\ts_cmp_eq_u32 s54, 0\n"
        "\ts_cbranch_scc1 LTa9\n"
        "\ts_sub_u32 s54, s54, 1\n"
        "\ts_branch LRa9\n"
        "LTa9:\n"
        "\ts_add_u32 m0, s67, 6144\n"
        "\ts_add_u32 s96, s73, 30720\n"
        "\tbuffer_load_dwordx4 v2, s[8:11], s96 offen sc1 lds\n"
        "\tbuffer_load_dwordx4 v2, s[8:11], s96 offen offset:1024 sc1 lds\n"
        "\ts_waitcnt vmcnt(0) lgkmcnt(0)\n"
        "\ts_nop 7\n"
        "\ts_branch LBa9\n"
        "LRa10:\n"
        "\ts_waitcnt vmcnt(0) lgkmcnt(0)\n"
        "\ts_nop 7\n"
        "\ts_sleep 1\n"
        "\ts_add_u32 m0, s67, 8192\n"
        "\ts_add_u32 s96, s73, 20480\n"
        "\tbuffer_load_dwordx4 v2, s[8:11], s96 offen sc1 lds\n"
        "\tbuffer_load_dwordx4 v2, s[8:11], s96 offen offset:1024 sc1 lds\n"
        "\ts_waitcnt vmcnt(0)\n"
        "\tds_read_b64_tr_b16 v[104:105], v3 offset:8192\n"
        "\tds_read_b64_tr_b16 v[106:107], v3 offset:9216\n"
        "\tds_read_b64_tr_b16 v[108:109], v4 offset:8192\n"
        "\tds_read_b64_tr_b16 v[110:111], v4 offset:9216\n"
        "\ts_waitcnt lgkmcnt(2)\n"
        "\tv_mfma_f32_16x16x32_bf16 v[184:187], a[224:227], v[104:107], v[192:195]\n"
        "\ts_waitcnt lgkmcnt(0)\n"
        "\tv_mfma_f32_16x16x32_bf16 v[188:191], a[224:227], v[108:111], v[196:199]\n"
        "\ts_nop 7\n"
        "\tv_cmp_u_f32 vcc, v184, v188\n"
        "\ts_nop 4\n"
        "\ts_cbranch_vccz LBa10\n"
        "\ts_cmp_eq_u32 s54, 0\n"
        "\ts_cbranch_scc1 LBa10\n"
        "\ts_sub_u32 s54, s54, 1\n"
        "\ts_branch LRa10\n"
        "LRa11:\n"
        "\ts_waitcnt vmcnt(0) lgkmcnt(0)\n"
        "\ts_nop 7\n"
        "\ts_sleep 1\n"
        "\ts_add_u32 m0, s67, 10240\n"
        "\ts_add_u32 s96, s73, 22528\n"
        "\tbuffer_load_dwordx4 v2, s[8:11], s96 offen sc1 lds\n"
        "\tbuffer_load_dwordx4 v2, s[8:11], s96 offen offset:1024 sc1 lds\n"
        "\ts_waitcnt vmcnt(0)\n"
        "\tds_read_b64_tr_b16 v[112:113], v3 offset:10240\n"
        "\tds_read_b64_tr_b16 v[114:115], v3 offset:11264\n"
        "\tds_read_b64_tr_b16 v[116:117], v4 offset:10240\n"
        "\tds_read_b64_tr_b16 v[118:119], v4 offset:11264\n"
        "\ts_waitcnt lgkmcnt(2)\n"
        "\tv_mfma_f32_16x16x32_bf16 v[192:195], a[224:227], v[112:115], v[184:187]\n"
        "\ts_waitcnt lgkmcnt(0)\n"
        "\tv_mfma_f32_16x16x32_bf16 v[196:199], a[224:227], v[116:119], v[188:191]\n"
        "\ts_nop 7\n"
        "\tv_cmp_u_f32 vcc, v192, v196\n"
        "\ts_nop 4\n"
        "\ts_cbranch_vccz LBa11\n"
        "\ts_cmp_eq_u32 s54, 0\n"
        "\ts_cbranch_scc1 LBa11\n"
        "\ts_sub_u32 s54, s54, 1\n"
        "\ts_branch LRa11\n"
        "LRa12:\n"
        "\ts_waitcnt vmcnt(0) lgkmcnt(0)\n"
        "\ts_nop 7\n"
        "\ts_sleep 1\n"
        "\ts_add_u32 m0, s67, 0\n"
        "\ts_add_u32 s96, s73, 24576\n"
        "\tbuffer_load_dwordx4 v2, s[8:11], s96 offen sc1 lds\n"
        "\tbuffer_load_dwordx4 v2, s[8:11], s96 offen offset:1024 sc1 lds\n"
        "\ts_waitcnt vmcnt(0)\n"
        "\tds_read_b64_tr_b16 v[120:121], v3 offset:0\n"
        "\tds_read_b64_tr_b16 v[122:123], v3 offset:1024\n"
        "\tds_read_b64_tr_b16 v[124:125], v4 offset:0\n"
        "\tds_read_b64_tr_b16 v[126:127], v4 offset:1024\n"
        "\ts_waitcnt lgkmcnt(2)\n"
        "\tv_mfma_f32_16x16x32_bf16 v[184:187], a[224:227], v[120:123], v[192:195]\n"
        "\ts_waitcnt lgkmcnt(0)\n"
        "\tv_mfma_f32_16x16x32_bf16 v[188:191], a[224:227], v[124:127], v[196:199]\n"
        "\ts_nop 7\n"
        "\tv_cmp_u_f32 vcc, v184, v188\n"
        "\ts_nop 4\n"
        "\ts_cbranch_vccz LBa12\n"
        "\ts_cmp_eq_u32 s54, 0\n"
        "\ts_cbranch_scc1 LBa12\n"
        "\ts_sub_u32 s54, s54, 1\n"
        "\ts_branch LRa12\n"
        "LRa13:\n"
        "\ts_waitcnt vmcnt(0) lgkmcnt(0)\n"
        "\ts_nop 7\n"
        "\ts_sleep 1\n"
        "\ts_add_u32 m0, s67, 2048\n"
        "\ts_add_u32 s96, s73, 26624\n"
        "\tbuffer_load_dwordx4 v2, s[8:11], s96 offen sc1 lds\n"
        "\tbuffer_load_dwordx4 v2, s[8:11], s96 offen offset:1024 sc1 lds\n"
        "\ts_waitcnt vmcnt(0)\n"
        "\tds_read_b64_tr_b16 v[128:129], v3 offset:2048\n"
        "\tds_read_b64_tr_b16 v[130:131], v3 offset:3072\n"
        "\tds_read_b64_tr_b16 v[132:133], v4 offset:2048\n"
        "\tds_read_b64_tr_b16 v[134:135], v4 offset:3072\n"
        "\ts_waitcnt lgkmcnt(2)\n"
        "\tv_mfma_f32_16x16x32_bf16 v[192:195], a[224:227], v[128:131], v[184:187]\n"
        "\ts_waitcnt lgkmcnt(0)\n"
        "\tv_mfma_f32_16x16x32_bf16 v[196:199], a[224:227], v[132:135], v[188:191]\n"
        "\ts_nop 7\n"
        "\tv_cmp_u_f32 vcc, v192, v196\n"
        "\ts_nop 4\n"
        "\ts_cbranch_vccz LBa13\n"
        "\ts_cmp_eq_u32 s54, 0\n"
        "\ts_cbranch_scc1 LBa13\n"
        "\ts_sub_u32 s54, s54, 1\n"
        "\ts_branch LRa13\n"
        "LRa14:\n"
        "\ts_waitcnt vmcnt(0) lgkmcnt(0)\n"
        "\ts_nop 7\n"
        "\ts_sleep 1\n"
        "\ts_add_u32 m0, s67, 4096\n"
        "\ts_add_u32 s96, s73, 28672\n"
        "\tbuffer_load_dwordx4 v2, s[8:11], s96 offen sc1 lds\n"
        "\tbuffer_load_dwordx4 v2, s[8:11], s96 offen offset:1024 sc1 lds\n"
        "\ts_waitcnt vmcnt(0)\n"
        "\tds_read_b64_tr_b16 v[136:137], v3 offset:4096\n"
        "\tds_read_b64_tr_b16 v[138:139], v3 offset:5120\n"
        "\tds_read_b64_tr_b16 v[140:141], v4 offset:4096\n"
        "\tds_read_b64_tr_b16 v[142:143], v4 offset:5120\n"
        "\ts_waitcnt lgkmcnt(2)\n"
        "\tv_mfma_f32_16x16x32_bf16 v[184:187], a[224:227], v[136:139], v[192:195]\n"
        "\ts_waitcnt lgkmcnt(0)\n"
        "\tv_mfma_f32_16x16x32_bf16 v[188:191], a[224:227], v[140:143], v[196:199]\n"
        "\ts_nop 7\n"
        "\tv_cmp_u_f32 vcc, v184, v188\n"
        "\ts_nop 4\n"
        "\ts_cbranch_vccz LBa14\n"
        "\ts_cmp_eq_u32 s54, 0\n"
        "\ts_cbranch_scc1 LBa14\n"
        "\ts_sub_u32 s54, s54, 1\n"
        "\ts_branch LRa14\n"
        "LRa15:\n"
        "\ts_waitcnt vmcnt(0) lgkmcnt(0)\n"
        "\ts_nop 7\n"
        "\ts_sleep 1\n"
        "\ts_add_u32 m0, s67, 6144\n"
        "\ts_add_u32 s96, s73, 30720\n"
        "\tbuffer_load_dwordx4 v2, s[8:11], s96 offen sc1 lds\n"
        "\tbuffer_load_dwordx4 v2, s[8:11], s96 offen offset:1024 sc1 lds\n"
        "\ts_waitcnt vmcnt(0)\n"
        "\tds_read_b64_tr_b16 v[144:145], v3 offset:6144\n"
        "\tds_read_b64_tr_b16 v[146:147], v3 offset:7168\n"
        "\tds_read_b64_tr_b16 v[148:149], v4 offset:6144\n"
        "\tds_read_b64_tr_b16 v[150:151], v4 offset:7168\n"
        "\ts_waitcnt lgkmcnt(2)\n"
        "\tv_mfma_f32_16x16x32_bf16 v[192:195], a[224:227], v[144:147], v[184:187]\n"
        "\ts_waitcnt lgkmcnt(0)\n"
        "\tv_mfma_f32_16x16x32_bf16 v[196:199], a[224:227], v[148:151], v[188:191]\n"
        "\ts_nop 7\n"
        "\tv_cmp_u_f32 vcc, v192, v196\n"
        "\ts_nop 4\n"
        "\ts_cbranch_vccz LBa15\n"
        "\ts_cmp_eq_u32 s54, 0\n"
        "\ts_cbranch_scc1 LBa15\n"
        "\ts_sub_u32 s54, s54, 1\n"
        "\ts_branch LRa15\n"
        "LRh0:\n"
        "\ts_waitcnt vmcnt(0) lgkmcnt(0)\n"
        "\ts_nop 7\n"
        "\ts_sleep 1\n"
        "\tbuffer_load_dwordx4 v[88:91], v1, s[8:11], s40 offen offset:0 sc1\n"
        "\tbuffer_load_dwordx4 v[92:95], v1, s[8:11], s45 offen offset:0 sc1\n"
        "\tbuffer_load_dwordx4 v[96:99], v1, s[8:11], s46 offen offset:0 sc1\n"
        "\tbuffer_load_dwordx4 v[100:103], v1, s[8:11], s48 offen offset:0 sc1\n"
        "\ts_waitcnt vmcnt(0)\n"
        "\tv_xor_b32 v22, v88, v252\n"
        "\tv_bitop3_b32 v22, v22, v89, v252 bitop3:0xf6\n"
        "\tv_bitop3_b32 v22, v22, v90, v252 bitop3:0xf6\n"
        "\tv_bitop3_b32 v22, v22, v91, v252 bitop3:0xf6\n"
        "\tv_bitop3_b32 v22, v22, v92, v252 bitop3:0xf6\n"
        "\tv_bitop3_b32 v22, v22, v93, v252 bitop3:0xf6\n"
        "\tv_bitop3_b32 v22, v22, v94, v252 bitop3:0xf6\n"
        "\tv_bitop3_b32 v22, v22, v95, v252 bitop3:0xf6\n"
        "\tv_bitop3_b32 v22, v22, v96, v252 bitop3:0xf6\n"
        "\tv_bitop3_b32 v22, v22, v97, v252 bitop3:0xf6\n"
        "\tv_bitop3_b32 v22, v22, v98, v252 bitop3:0xf6\n"
        "\tv_bitop3_b32 v22, v22, v99, v252 bitop3:0xf6\n"
        "\tv_bitop3_b32 v22, v22, v100, v252 bitop3:0xf6\n"
        "\tv_bitop3_b32 v22, v22, v101, v252 bitop3:0xf6\n"
        "\tv_bitop3_b32 v22, v22, v102, v252 bitop3:0xf6\n"
        "\tv_bitop3_b32 v22, v22, v103, v252 bitop3:0xf6\n"
        "\tv_and_b32 v22, s30, v22\n"
        "\tv_cmp_ne_u32 vcc, 0, v22\n"
        "\ts_nop 4\n"
        "\ts_cbranch_vccz LBh0\n"
        "\ts_cmp_eq_u32 s54, 0\n"
        "\ts_cbranch_scc1 LBh0\n"
        "\ts_sub_u32 s54, s54, 1\n"
        "\ts_branch LRh0\n"
        "LRh1:\n"
        "\ts_waitcnt vmcnt(0) lgkmcnt(0)\n"
        "\ts_nop 7\n"
        "\ts_sleep 1\n"
        "\tbuffer_load_dwordx4 v[104:107], v1, s[8:11], s49 offen offset:0 sc1\n"
        "\tbuffer_load_dwordx4 v[108:111], v1, s[8:11], s50 offen offset:0 sc1\n"
        "\tbuffer_load_dwordx4 v[112:115], v1, s[8:11], s51 offen offset:0 sc1\n"
        "\tbuffer_load_dwordx4 v[116:119], v1, s[8:11], s63 offen offset:0 sc1\n"
        "\ts_waitcnt vmcnt(0)\n"
        "\tv_xor_b32 v22, v104, v252\n"
        "\tv_bitop3_b32 v22, v22, v105, v252 bitop3:0xf6\n"
        "\tv_bitop3_b32 v22, v22, v106, v252 bitop3:0xf6\n"
        "\tv_bitop3_b32 v22, v22, v107, v252 bitop3:0xf6\n"
        "\tv_bitop3_b32 v22, v22, v108, v252 bitop3:0xf6\n"
        "\tv_bitop3_b32 v22, v22, v109, v252 bitop3:0xf6\n"
        "\tv_bitop3_b32 v22, v22, v110, v252 bitop3:0xf6\n"
        "\tv_bitop3_b32 v22, v22, v111, v252 bitop3:0xf6\n"
        "\tv_bitop3_b32 v22, v22, v112, v252 bitop3:0xf6\n"
        "\tv_bitop3_b32 v22, v22, v113, v252 bitop3:0xf6\n"
        "\tv_bitop3_b32 v22, v22, v114, v252 bitop3:0xf6\n"
        "\tv_bitop3_b32 v22, v22, v115, v252 bitop3:0xf6\n"
        "\tv_bitop3_b32 v22, v22, v116, v252 bitop3:0xf6\n"
        "\tv_bitop3_b32 v22, v22, v117, v252 bitop3:0xf6\n"
        "\tv_bitop3_b32 v22, v22, v118, v252 bitop3:0xf6\n"
        "\tv_bitop3_b32 v22, v22, v119, v252 bitop3:0xf6\n"
        "\tv_and_b32 v22, s30, v22\n"
        "\tv_cmp_ne_u32 vcc, 0, v22\n"
        "\ts_nop 4\n"
        "\ts_cbranch_vccz LBh1\n"
        "\ts_cmp_eq_u32 s54, 0\n"
        "\ts_cbranch_scc1 LBh1\n"
        "\ts_sub_u32 s54, s54, 1\n"
        "\ts_branch LRh1\n"
        "LRh2:\n"
        "\ts_waitcnt vmcnt(0) lgkmcnt(0)\n"
        "\ts_nop 7\n"
        "\ts_sleep 1\n"
        "\tbuffer_load_dwordx4 v[120:123], v1, s[8:11], s65 offen offset:0 sc1\n"
        "\tbuffer_load_dwordx4 v[124:127], v1, s[8:11], s68 offen offset:0 sc1\n"
        "\tbuffer_load_dwordx4 v[128:131], v1, s[8:11], s69 offen offset:0 sc1\n"
        "\tbuffer_load_dwordx4 v[132:135], v1, s[8:11], s97 offen offset:0 sc1\n"
        "\ts_waitcnt vmcnt(0)\n"
        "\tv_xor_b32 v22, v120, v252\n"
        "\tv_bitop3_b32 v22, v22, v121, v252 bitop3:0xf6\n"
        "\tv_bitop3_b32 v22, v22, v122, v252 bitop3:0xf6\n"
        "\tv_bitop3_b32 v22, v22, v123, v252 bitop3:0xf6\n"
        "\tv_bitop3_b32 v22, v22, v124, v252 bitop3:0xf6\n"
        "\tv_bitop3_b32 v22, v22, v125, v252 bitop3:0xf6\n"
        "\tv_bitop3_b32 v22, v22, v126, v252 bitop3:0xf6\n"
        "\tv_bitop3_b32 v22, v22, v127, v252 bitop3:0xf6\n"
        "\tv_bitop3_b32 v22, v22, v128, v252 bitop3:0xf6\n"
        "\tv_bitop3_b32 v22, v22, v129, v252 bitop3:0xf6\n"
        "\tv_bitop3_b32 v22, v22, v130, v252 bitop3:0xf6\n"
        "\tv_bitop3_b32 v22, v22, v131, v252 bitop3:0xf6\n"
        "\tv_bitop3_b32 v22, v22, v132, v252 bitop3:0xf6\n"
        "\tv_bitop3_b32 v22, v22, v133, v252 bitop3:0xf6\n"
        "\tv_bitop3_b32 v22, v22, v134, v252 bitop3:0xf6\n"
        "\tv_bitop3_b32 v22, v22, v135, v252 bitop3:0xf6\n"
        "\tv_and_b32 v22, s30, v22\n"
        "\tv_cmp_ne_u32 vcc, 0, v22\n"
        "\ts_nop 4\n"
        "\ts_cbranch_vccz LBh2\n"
        "\ts_cmp_eq_u32 s54, 0\n"
        "\ts_cbranch_scc1 LBh2\n"
        "\ts_sub_u32 s54, s54, 1\n"
        "\ts_branch LRh2\n"
        "LRh3:\n"
        "\ts_waitcnt vmcnt(0) lgkmcnt(0)\n"
        "\ts_nop 7\n"
        "\ts_sleep 1\n"
        "\tbuffer_load_dwordx4 v[136:139], v1, s[8:11], s98 offen offset:0 sc1\n"
        "\tbuffer_load_dwordx4 v[140:143], v1, s[8:11], s99 offen offset:0 sc1\n"
        "\tbuffer_load_dwordx4 v[144:147], v1, s[8:11], s74 offen offset:0 sc1\n"
        "\tbuffer_load_dwordx4 v[148:151], v1, s[8:11], s76 offen offset:0 sc1\n"
        "\ts_waitcnt vmcnt(0)\n"
        "\tv_xor_b32 v22, v136, v252\n"
        "\tv_bitop3_b32 v22, v22, v137, v252 bitop3:0xf6\n"
        "\tv_bitop3_b32 v22, v22, v138, v252 bitop3:0xf6\n"
        "\tv_bitop3_b32 v22, v22, v139, v252 bitop3:0xf6\n"
        "\tv_bitop3_b32 v22, v22, v140, v252 bitop3:0xf6\n"
        "\tv_bitop3_b32 v22, v22, v141, v252 bitop3:0xf6\n"
        "\tv_bitop3_b32 v22, v22, v142, v252 bitop3:0xf6\n"
        "\tv_bitop3_b32 v22, v22, v143, v252 bitop3:0xf6\n"
        "\tv_bitop3_b32 v22, v22, v144, v252 bitop3:0xf6\n"
        "\tv_bitop3_b32 v22, v22, v145, v252 bitop3:0xf6\n"
        "\tv_bitop3_b32 v22, v22, v146, v252 bitop3:0xf6\n"
        "\tv_bitop3_b32 v22, v22, v147, v252 bitop3:0xf6\n"
        "\tv_bitop3_b32 v22, v22, v148, v252 bitop3:0xf6\n"
        "\tv_bitop3_b32 v22, v22, v149, v252 bitop3:0xf6\n"
        "\tv_bitop3_b32 v22, v22, v150, v252 bitop3:0xf6\n"
        "\tv_bitop3_b32 v22, v22, v151, v252 bitop3:0xf6\n"
        "\tv_and_b32 v22, s30, v22\n"
        "\tv_cmp_ne_u32 vcc, 0, v22\n"
        "\ts_nop 4\n"
        "\ts_cbranch_vccz LBh3\n"
        "\ts_cmp_eq_u32 s54, 0\n"
        "\ts_cbranch_scc1 LBh3\n"
        "\ts_sub_u32 s54, s54, 1\n"
        "\ts_branch LRh3\n"
        "LRh4:\n"
        "\ts_waitcnt vmcnt(0) lgkmcnt(0)\n"
        "\ts_nop 7\n"
        "\ts_sleep 1\n"
        "\tbuffer_load_dwordx4 v[88:91], v1, s[8:11], s40 offen offset:1024 sc1\n"
        "\tbuffer_load_dwordx4 v[92:95], v1, s[8:11], s45 offen offset:1024 sc1\n"
        "\tbuffer_load_dwordx4 v[96:99], v1, s[8:11], s46 offen offset:1024 sc1\n"
        "\tbuffer_load_dwordx4 v[100:103], v1, s[8:11], s48 offen offset:1024 sc1\n"
        "\ts_waitcnt vmcnt(0)\n"
        "\tv_xor_b32 v22, v88, v252\n"
        "\tv_bitop3_b32 v22, v22, v89, v252 bitop3:0xf6\n"
        "\tv_bitop3_b32 v22, v22, v90, v252 bitop3:0xf6\n"
        "\tv_bitop3_b32 v22, v22, v91, v252 bitop3:0xf6\n"
        "\tv_bitop3_b32 v22, v22, v92, v252 bitop3:0xf6\n"
        "\tv_bitop3_b32 v22, v22, v93, v252 bitop3:0xf6\n"
        "\tv_bitop3_b32 v22, v22, v94, v252 bitop3:0xf6\n"
        "\tv_bitop3_b32 v22, v22, v95, v252 bitop3:0xf6\n"
        "\tv_bitop3_b32 v22, v22, v96, v252 bitop3:0xf6\n"
        "\tv_bitop3_b32 v22, v22, v97, v252 bitop3:0xf6\n"
        "\tv_bitop3_b32 v22, v22, v98, v252 bitop3:0xf6\n"
        "\tv_bitop3_b32 v22, v22, v99, v252 bitop3:0xf6\n"
        "\tv_bitop3_b32 v22, v22, v100, v252 bitop3:0xf6\n"
        "\tv_bitop3_b32 v22, v22, v101, v252 bitop3:0xf6\n"
        "\tv_bitop3_b32 v22, v22, v102, v252 bitop3:0xf6\n"
        "\tv_bitop3_b32 v22, v22, v103, v252 bitop3:0xf6\n"
        "\tv_and_b32 v22, s30, v22\n"
        "\tv_cmp_ne_u32 vcc, 0, v22\n"
        "\ts_nop 4\n"
        "\ts_cbranch_vccz LBh4\n"
        "\ts_cmp_eq_u32 s54, 0\n"
        "\ts_cbranch_scc1 LBh4\n"
        "\ts_sub_u32 s54, s54, 1\n"
        "\ts_branch LRh4\n"
        "LRh5:\n"
        "\ts_waitcnt vmcnt(0) lgkmcnt(0)\n"
        "\ts_nop 7\n"
        "\ts_sleep 1\n"
        "\tbuffer_load_dwordx4 v[104:107], v1, s[8:11], s49 offen offset:1024 sc1\n"
        "\tbuffer_load_dwordx4 v[108:111], v1, s[8:11], s50 offen offset:1024 sc1\n"
        "\tbuffer_load_dwordx4 v[112:115], v1, s[8:11], s51 offen offset:1024 sc1\n"
        "\tbuffer_load_dwordx4 v[116:119], v1, s[8:11], s63 offen offset:1024 sc1\n"
        "\ts_waitcnt vmcnt(0)\n"
        "\tv_xor_b32 v22, v104, v252\n"
        "\tv_bitop3_b32 v22, v22, v105, v252 bitop3:0xf6\n"
        "\tv_bitop3_b32 v22, v22, v106, v252 bitop3:0xf6\n"
        "\tv_bitop3_b32 v22, v22, v107, v252 bitop3:0xf6\n"
        "\tv_bitop3_b32 v22, v22, v108, v252 bitop3:0xf6\n"
        "\tv_bitop3_b32 v22, v22, v109, v252 bitop3:0xf6\n"
        "\tv_bitop3_b32 v22, v22, v110, v252 bitop3:0xf6\n"
        "\tv_bitop3_b32 v22, v22, v111, v252 bitop3:0xf6\n"
        "\tv_bitop3_b32 v22, v22, v112, v252 bitop3:0xf6\n"
        "\tv_bitop3_b32 v22, v22, v113, v252 bitop3:0xf6\n"
        "\tv_bitop3_b32 v22, v22, v114, v252 bitop3:0xf6\n"
        "\tv_bitop3_b32 v22, v22, v115, v252 bitop3:0xf6\n"
        "\tv_bitop3_b32 v22, v22, v116, v252 bitop3:0xf6\n"
        "\tv_bitop3_b32 v22, v22, v117, v252 bitop3:0xf6\n"
        "\tv_bitop3_b32 v22, v22, v118, v252 bitop3:0xf6\n"
        "\tv_bitop3_b32 v22, v22, v119, v252 bitop3:0xf6\n"
        "\tv_and_b32 v22, s30, v22\n"
        "\tv_cmp_ne_u32 vcc, 0, v22\n"
        "\ts_nop 4\n"
        "\ts_cbranch_vccz LBh5\n"
        "\ts_cmp_eq_u32 s54, 0\n"
        "\ts_cbranch_scc1 LBh5\n"
        "\ts_sub_u32 s54, s54, 1\n"
        "\ts_branch LRh5\n"
        "LRh6:\n"
        "\ts_waitcnt vmcnt(0) lgkmcnt(0)\n"
        "\ts_nop 7\n"
        "\ts_sleep 1\n"
        "\tbuffer_load_dwordx4 v[120:123], v1, s[8:11], s65 offen offset:1024 sc1\n"
        "\tbuffer_load_dwordx4 v[124:127], v1, s[8:11], s68 offen offset:1024 sc1\n"
        "\tbuffer_load_dwordx4 v[128:131], v1, s[8:11], s69 offen offset:1024 sc1\n"
        "\tbuffer_load_dwordx4 v[132:135], v1, s[8:11], s97 offen offset:1024 sc1\n"
        "\ts_waitcnt vmcnt(0)\n"
        "\tv_xor_b32 v22, v120, v252\n"
        "\tv_bitop3_b32 v22, v22, v121, v252 bitop3:0xf6\n"
        "\tv_bitop3_b32 v22, v22, v122, v252 bitop3:0xf6\n"
        "\tv_bitop3_b32 v22, v22, v123, v252 bitop3:0xf6\n"
        "\tv_bitop3_b32 v22, v22, v124, v252 bitop3:0xf6\n"
        "\tv_bitop3_b32 v22, v22, v125, v252 bitop3:0xf6\n"
        "\tv_bitop3_b32 v22, v22, v126, v252 bitop3:0xf6\n"
        "\tv_bitop3_b32 v22, v22, v127, v252 bitop3:0xf6\n"
        "\tv_bitop3_b32 v22, v22, v128, v252 bitop3:0xf6\n"
        "\tv_bitop3_b32 v22, v22, v129, v252 bitop3:0xf6\n"
        "\tv_bitop3_b32 v22, v22, v130, v252 bitop3:0xf6\n"
        "\tv_bitop3_b32 v22, v22, v131, v252 bitop3:0xf6\n"
        "\tv_bitop3_b32 v22, v22, v132, v252 bitop3:0xf6\n"
        "\tv_bitop3_b32 v22, v22, v133, v252 bitop3:0xf6\n"
        "\tv_bitop3_b32 v22, v22, v134, v252 bitop3:0xf6\n"
        "\tv_bitop3_b32 v22, v22, v135, v252 bitop3:0xf6\n"
        "\tv_and_b32 v22, s30, v22\n"
        "\tv_cmp_ne_u32 vcc, 0, v22\n"
        "\ts_nop 4\n"
        "\ts_cbranch_vccz LBh6\n"
        "\ts_cmp_eq_u32 s54, 0\n"
        "\ts_cbranch_scc1 LBh6\n"
        "\ts_sub_u32 s54, s54, 1\n"
        "\ts_branch LRh6\n"
        "LRh7:\n"
        "\ts_waitcnt vmcnt(0) lgkmcnt(0)\n"
        "\ts_nop 7\n"
        "\ts_sleep 1\n"
        "\tbuffer_load_dwordx4 v[136:139], v1, s[8:11], s78 offen offset:0 sc1\n"
        "\tbuffer_load_dwordx4 v[140:143], v1, s[8:11], s78 offen offset:1024 sc1\n"
        "\tbuffer_load_dwordx4 v[144:147], v1, s[8:11], s78 offen offset:2048 sc1\n"
        "\tbuffer_load_dwordx4 v[148:151], v1, s[8:11], s78 offen offset:3072 sc1\n"
        "\ts_waitcnt vmcnt(0)\n"
        "\tv_xor_b32 v22, v136, v252\n"
        "\tv_bitop3_b32 v22, v22, v137, v252 bitop3:0xf6\n"
        "\tv_bitop3_b32 v22, v22, v138, v252 bitop3:0xf6\n"
        "\tv_bitop3_b32 v22, v22, v139, v252 bitop3:0xf6\n"
        "\tv_bitop3_b32 v22, v22, v140, v252 bitop3:0xf6\n"
        "\tv_bitop3_b32 v22, v22, v141, v252 bitop3:0xf6\n"
        "\tv_bitop3_b32 v22, v22, v142, v252 bitop3:0xf6\n"
        "\tv_bitop3_b32 v22, v22, v143, v252 bitop3:0xf6\n"
        "\tv_bitop3_b32 v22, v22, v144, v252 bitop3:0xf6\n"
        "\tv_bitop3_b32 v22, v22, v145, v252 bitop3:0xf6\n"
        "\tv_bitop3_b32 v22, v22, v146, v252 bitop3:0xf6\n"
        "\tv_bitop3_b32 v22, v22, v147, v252 bitop3:0xf6\n"
        "\tv_bitop3_b32 v22, v22, v148, v252 bitop3:0xf6\n"
        "\tv_bitop3_b32 v22, v22, v149, v252 bitop3:0xf6\n"
        "\tv_bitop3_b32 v22, v22, v150, v252 bitop3:0xf6\n"
        "\tv_bitop3_b32 v22, v22, v151, v252 bitop3:0xf6\n"
        "\tv_and_b32 v22, s30, v22\n"
        "\tv_cmp_ne_u32 vcc, 0, v22\n"
        "\ts_nop 4\n"
        "\ts_cbranch_vccz LBh7\n"
        "\ts_cmp_eq_u32 s54, 0\n"
        "\ts_cbranch_scc1 LBh7\n"
        "\ts_sub_u32 s54, s54, 1\n"
        "\ts_branch LRh7\n"
        "LRh8:\n"
        "\ts_waitcnt vmcnt(0) lgkmcnt(0)\n"
        "\ts_nop 7\n"
        "\ts_sleep 1\n"
        "\tbuffer_load_dwordx4 v[88:91], v1, s[8:11], s40 offen offset:2048 sc1\n"
        "\tbuffer_load_dwordx4 v[92:95], v1, s[8:11], s45 offen offset:2048 sc1\n"
        "\tbuffer_load_dwordx4 v[96:99], v1, s[8:11], s46 offen offset:2048 sc1\n"
        "\tbuffer_load_dwordx4 v[100:103], v1, s[8:11], s48 offen offset:2048 sc1\n"
        "\ts_waitcnt vmcnt(0)\n"
        "\tv_xor_b32 v22, v88, v252\n"
        "\tv_bitop3_b32 v22, v22, v89, v252 bitop3:0xf6\n"
        "\tv_bitop3_b32 v22, v22, v90, v252 bitop3:0xf6\n"
        "\tv_bitop3_b32 v22, v22, v91, v252 bitop3:0xf6\n"
        "\tv_bitop3_b32 v22, v22, v92, v252 bitop3:0xf6\n"
        "\tv_bitop3_b32 v22, v22, v93, v252 bitop3:0xf6\n"
        "\tv_bitop3_b32 v22, v22, v94, v252 bitop3:0xf6\n"
        "\tv_bitop3_b32 v22, v22, v95, v252 bitop3:0xf6\n"
        "\tv_bitop3_b32 v22, v22, v96, v252 bitop3:0xf6\n"
        "\tv_bitop3_b32 v22, v22, v97, v252 bitop3:0xf6\n"
        "\tv_bitop3_b32 v22, v22, v98, v252 bitop3:0xf6\n"
        "\tv_bitop3_b32 v22, v22, v99, v252 bitop3:0xf6\n"
        "\tv_bitop3_b32 v22, v22, v100, v252 bitop3:0xf6\n"
        "\tv_bitop3_b32 v22, v22, v101, v252 bitop3:0xf6\n"
        "\tv_bitop3_b32 v22, v22, v102, v252 bitop3:0xf6\n"
        "\tv_bitop3_b32 v22, v22, v103, v252 bitop3:0xf6\n"
        "\tv_and_b32 v22, s30, v22\n"
        "\tv_cmp_ne_u32 vcc, 0, v22\n"
        "\ts_nop 4\n"
        "\ts_cbranch_vccz LBh8\n"
        "\ts_cmp_eq_u32 s54, 0\n"
        "\ts_cbranch_scc1 LBh8\n"
        "\ts_sub_u32 s54, s54, 1\n"
        "\ts_branch LRh8\n"
        "LRh9:\n"
        "\ts_waitcnt vmcnt(0) lgkmcnt(0)\n"
        "\ts_nop 7\n"
        "\ts_sleep 1\n"
        "\tbuffer_load_dwordx4 v[104:107], v1, s[8:11], s49 offen offset:2048 sc1\n"
        "\tbuffer_load_dwordx4 v[108:111], v1, s[8:11], s50 offen offset:2048 sc1\n"
        "\tbuffer_load_dwordx4 v[112:115], v1, s[8:11], s51 offen offset:2048 sc1\n"
        "\tbuffer_load_dwordx4 v[116:119], v1, s[8:11], s63 offen offset:2048 sc1\n"
        "\ts_waitcnt vmcnt(0)\n"
        "\tv_xor_b32 v22, v104, v252\n"
        "\tv_bitop3_b32 v22, v22, v105, v252 bitop3:0xf6\n"
        "\tv_bitop3_b32 v22, v22, v106, v252 bitop3:0xf6\n"
        "\tv_bitop3_b32 v22, v22, v107, v252 bitop3:0xf6\n"
        "\tv_bitop3_b32 v22, v22, v108, v252 bitop3:0xf6\n"
        "\tv_bitop3_b32 v22, v22, v109, v252 bitop3:0xf6\n"
        "\tv_bitop3_b32 v22, v22, v110, v252 bitop3:0xf6\n"
        "\tv_bitop3_b32 v22, v22, v111, v252 bitop3:0xf6\n"
        "\tv_bitop3_b32 v22, v22, v112, v252 bitop3:0xf6\n"
        "\tv_bitop3_b32 v22, v22, v113, v252 bitop3:0xf6\n"
        "\tv_bitop3_b32 v22, v22, v114, v252 bitop3:0xf6\n"
        "\tv_bitop3_b32 v22, v22, v115, v252 bitop3:0xf6\n"
        "\tv_bitop3_b32 v22, v22, v116, v252 bitop3:0xf6\n"
        "\tv_bitop3_b32 v22, v22, v117, v252 bitop3:0xf6\n"
        "\tv_bitop3_b32 v22, v22, v118, v252 bitop3:0xf6\n"
        "\tv_bitop3_b32 v22, v22, v119, v252 bitop3:0xf6\n"
        "\tv_and_b32 v22, s30, v22\n"
        "\tv_cmp_ne_u32 vcc, 0, v22\n"
        "\ts_nop 4\n"
        "\ts_cbranch_vccz LBh9\n"
        "\ts_cmp_eq_u32 s54, 0\n"
        "\ts_cbranch_scc1 LBh9\n"
        "\ts_sub_u32 s54, s54, 1\n"
        "\ts_branch LRh9\n"
        "LRh10:\n"
        "\ts_waitcnt vmcnt(0) lgkmcnt(0)\n"
        "\ts_nop 7\n"
        "\ts_sleep 1\n"
        "\tbuffer_load_dwordx4 v[120:123], v1, s[8:11], s79 offen offset:0 sc1\n"
        "\tbuffer_load_dwordx4 v[124:127], v1, s[8:11], s79 offen offset:1024 sc1\n"
        "\tbuffer_load_dwordx4 v[128:131], v1, s[8:11], s79 offen offset:2048 sc1\n"
        "\tbuffer_load_dwordx4 v[132:135], v1, s[8:11], s79 offen offset:3072 sc1\n"
        "\ts_waitcnt vmcnt(0)\n"
        "\tv_xor_b32 v22, v120, v252\n"
        "\tv_bitop3_b32 v22, v22, v121, v252 bitop3:0xf6\n"
        "\tv_bitop3_b32 v22, v22, v122, v252 bitop3:0xf6\n"
        "\tv_bitop3_b32 v22, v22, v123, v252 bitop3:0xf6\n"
        "\tv_bitop3_b32 v22, v22, v124, v252 bitop3:0xf6\n"
        "\tv_bitop3_b32 v22, v22, v125, v252 bitop3:0xf6\n"
        "\tv_bitop3_b32 v22, v22, v126, v252 bitop3:0xf6\n"
        "\tv_bitop3_b32 v22, v22, v127, v252 bitop3:0xf6\n"
        "\tv_bitop3_b32 v22, v22, v128, v252 bitop3:0xf6\n"
        "\tv_bitop3_b32 v22, v22, v129, v252 bitop3:0xf6\n"
        "\tv_bitop3_b32 v22, v22, v130, v252 bitop3:0xf6\n"
        "\tv_bitop3_b32 v22, v22, v131, v252 bitop3:0xf6\n"
        "\tv_bitop3_b32 v22, v22, v132, v252 bitop3:0xf6\n"
        "\tv_bitop3_b32 v22, v22, v133, v252 bitop3:0xf6\n"
        "\tv_bitop3_b32 v22, v22, v134, v252 bitop3:0xf6\n"
        "\tv_bitop3_b32 v22, v22, v135, v252 bitop3:0xf6\n"
        "\tv_and_b32 v22, s30, v22\n"
        "\tv_cmp_ne_u32 vcc, 0, v22\n"
        "\ts_nop 4\n"
        "\ts_cbranch_vccz LBh10\n"
        "\ts_cmp_eq_u32 s54, 0\n"
        "\ts_cbranch_scc1 LBh10\n"
        "\ts_sub_u32 s54, s54, 1\n"
        "\ts_branch LRh10\n"
        "LRh11:\n"
        "\ts_waitcnt vmcnt(0) lgkmcnt(0)\n"
        "\ts_nop 7\n"
        "\ts_sleep 1\n"
        "\tbuffer_load_dwordx4 v[136:139], v1, s[8:11], s65 offen offset:2048 sc1\n"
        "\tbuffer_load_dwordx4 v[140:143], v1, s[8:11], s68 offen offset:2048 sc1\n"
        "\tbuffer_load_dwordx4 v[144:147], v1, s[8:11], s69 offen offset:2048 sc1\n"
        "\tbuffer_load_dwordx4 v[148:151], v1, s[8:11], s97 offen offset:2048 sc1\n"
        "\ts_waitcnt vmcnt(0)\n"
        "\tv_xor_b32 v22, v136, v252\n"
        "\tv_bitop3_b32 v22, v22, v137, v252 bitop3:0xf6\n"
        "\tv_bitop3_b32 v22, v22, v138, v252 bitop3:0xf6\n"
        "\tv_bitop3_b32 v22, v22, v139, v252 bitop3:0xf6\n"
        "\tv_bitop3_b32 v22, v22, v140, v252 bitop3:0xf6\n"
        "\tv_bitop3_b32 v22, v22, v141, v252 bitop3:0xf6\n"
        "\tv_bitop3_b32 v22, v22, v142, v252 bitop3:0xf6\n"
        "\tv_bitop3_b32 v22, v22, v143, v252 bitop3:0xf6\n"
        "\tv_bitop3_b32 v22, v22, v144, v252 bitop3:0xf6\n"
        "\tv_bitop3_b32 v22, v22, v145, v252 bitop3:0xf6\n"
        "\tv_bitop3_b32 v22, v22, v146, v252 bitop3:0xf6\n"
        "\tv_bitop3_b32 v22, v22, v147, v252 bitop3:0xf6\n"
        "\tv_bitop3_b32 v22, v22, v148, v252 bitop3:0xf6\n"
        "\tv_bitop3_b32 v22, v22, v149, v252 bitop3:0xf6\n"
        "\tv_bitop3_b32 v22, v22, v150, v252 bitop3:0xf6\n"
        "\tv_bitop3_b32 v22, v22, v151, v252 bitop3:0xf6\n"
        "\tv_and_b32 v22, s30, v22\n"
        "\tv_cmp_ne_u32 vcc, 0, v22\n"
        "\ts_nop 4\n"
        "\ts_cbranch_vccz LBh11\n"
        "\ts_cmp_eq_u32 s54, 0\n"
        "\ts_cbranch_scc1 LBh11\n"
        "\ts_sub_u32 s54, s54, 1\n"
        "\ts_branch LRh11\n"
        "LRh12:\n"
        "\ts_waitcnt vmcnt(0) lgkmcnt(0)\n"
        "\ts_nop 7\n"
        "\ts_sleep 1\n"
        "\tbuffer_load_dwordx4 v[88:91], v1, s[8:11], s98 offen offset:1024 sc1\n"
        "\tbuffer_load_dwordx4 v[92:95], v1, s[8:11], s99 offen offset:1024 sc1\n"
        "\tbuffer_load_dwordx4 v[96:99], v1, s[8:11], s74 offen offset:1024 sc1\n"
        "\tbuffer_load_dwordx4 v[100:103], v1, s[8:11], s76 offen offset:1024 sc1\n"
        "\ts_waitcnt vmcnt(0)\n"
        "\tv_xor_b32 v22, v88, v252\n"
        "\tv_bitop3_b32 v22, v22, v89, v252 bitop3:0xf6\n"
        "\tv_bitop3_b32 v22, v22, v90, v252 bitop3:0xf6\n"
        "\tv_bitop3_b32 v22, v22, v91, v252 bitop3:0xf6\n"
        "\tv_bitop3_b32 v22, v22, v92, v252 bitop3:0xf6\n"
        "\tv_bitop3_b32 v22, v22, v93, v252 bitop3:0xf6\n"
        "\tv_bitop3_b32 v22, v22, v94, v252 bitop3:0xf6\n"
        "\tv_bitop3_b32 v22, v22, v95, v252 bitop3:0xf6\n"
        "\tv_bitop3_b32 v22, v22, v96, v252 bitop3:0xf6\n"
        "\tv_bitop3_b32 v22, v22, v97, v252 bitop3:0xf6\n"
        "\tv_bitop3_b32 v22, v22, v98, v252 bitop3:0xf6\n"
        "\tv_bitop3_b32 v22, v22, v99, v252 bitop3:0xf6\n"
        "\tv_bitop3_b32 v22, v22, v100, v252 bitop3:0xf6\n"
        "\tv_bitop3_b32 v22, v22, v101, v252 bitop3:0xf6\n"
        "\tv_bitop3_b32 v22, v22, v102, v252 bitop3:0xf6\n"
        "\tv_bitop3_b32 v22, v22, v103, v252 bitop3:0xf6\n"
        "\tv_and_b32 v22, s30, v22\n"
        "\tv_cmp_ne_u32 vcc, 0, v22\n"
        "\ts_nop 4\n"
        "\ts_cbranch_vccz LBh12\n"
        "\ts_cmp_eq_u32 s54, 0\n"
        "\ts_cbranch_scc1 LBh12\n"
        "\ts_sub_u32 s54, s54, 1\n"
        "\ts_branch LRh12\n"
        "LRh13:\n"
        "\ts_waitcnt vmcnt(0) lgkmcnt(0)\n"
        "\ts_nop 7\n"
        "\ts_sleep 1\n"
        "\tbuffer_load_dwordx4 v[104:107], v1, s[8:11], s98 offen offset:2048 sc1\n"
        "\tbuffer_load_dwordx4 v[108:111], v1, s[8:11], s99 offen offset:2048 sc1\n"
        "\tbuffer_load_dwordx4 v[112:115], v1, s[8:11], s74 offen offset:2048 sc1\n"
        "\tbuffer_load_dwordx4 v[116:119], v1, s[8:11], s76 offen offset:2048 sc1\n"
        "\ts_waitcnt vmcnt(0)\n"
        "\tv_xor_b32 v22, v104, v252\n"
        "\tv_bitop3_b32 v22, v22, v105, v252 bitop3:0xf6\n"
        "\tv_bitop3_b32 v22, v22, v106, v252 bitop3:0xf6\n"
        "\tv_bitop3_b32 v22, v22, v107, v252 bitop3:0xf6\n"
        "\tv_bitop3_b32 v22, v22, v108, v252 bitop3:0xf6\n"
        "\tv_bitop3_b32 v22, v22, v109, v252 bitop3:0xf6\n"
        "\tv_bitop3_b32 v22, v22, v110, v252 bitop3:0xf6\n"
        "\tv_bitop3_b32 v22, v22, v111, v252 bitop3:0xf6\n"
        "\tv_bitop3_b32 v22, v22, v112, v252 bitop3:0xf6\n"
        "\tv_bitop3_b32 v22, v22, v113, v252 bitop3:0xf6\n"
        "\tv_bitop3_b32 v22, v22, v114, v252 bitop3:0xf6\n"
        "\tv_bitop3_b32 v22, v22, v115, v252 bitop3:0xf6\n"
        "\tv_bitop3_b32 v22, v22, v116, v252 bitop3:0xf6\n"
        "\tv_bitop3_b32 v22, v22, v117, v252 bitop3:0xf6\n"
        "\tv_bitop3_b32 v22, v22, v118, v252 bitop3:0xf6\n"
        "\tv_bitop3_b32 v22, v22, v119, v252 bitop3:0xf6\n"
        "\tv_and_b32 v22, s30, v22\n"
        "\tv_cmp_ne_u32 vcc, 0, v22\n"
        "\ts_nop 4\n"
        "\ts_cbranch_vccz LBh13\n"
        "\ts_cmp_eq_u32 s54, 0\n"
        "\ts_cbranch_scc1 LBh13\n"
        "\ts_sub_u32 s54, s54, 1\n"
        "\ts_branch LRh13\n"
        "LEND:\n"
        : "+{v0}"(tid)
        : "{s[0:1]}"(__builtin_amdgcn_kernarg_segment_ptr()), "{s2}"(blockIdx.x)
        : "v1", "v2", "v3", "v4", "v5", "v6", "v7", "v8", "v9", "v10", "v11", "v12", "v13", "v14", "v15", "v16", "v17", "v18", "v19", "v20", "v21", "v22", "v23", "v24", "v25", "v26", "v27", "v28", "v29", "v30", "v31", "v32", "v33", "v34", "v35", "v36", "v37", "v38", "v39", "v40", "v41", "v42", "v43", "v44", "v45", "v46", "v47", "v48", "v49", "v50", "v51", "v52", "v53", "v54", "v55", "v56", "v57", "v58", "v59", "v60", "v61", "v62", "v63", "v64", "v65", "v66", "v67", "v68", "v69", "v70", "v71", "v72", "v73", "v74", "v75", "v76", "v77", "v78", "v79", "v80", "v81", "v82", "v83", "v84", "v85", "v86", "v87", "v88", "v89", "v90", "v91", "v92", "v93", "v94", "v95", "v96", "v97", "v98", "v99", "v100", "v101", "v102", "v103", "v104", "v105", "v106", "v107", "v108", "v109", "v110", "v111", "v112", "v113", "v114", "v115", "v116", "v117", "v118", "v119", "v120", "v121", "v122", "v123", "v124", "v125", "v126", "v127", "v128", "v129", "v130", "v131", "v132", "v133", "v134", "v135", "v136", "v137", "v138", "v139", "v140", "v141", "v142", "v143", "v144", "v145", "v146", "v147", "v148", "v149", "v150", "v151", "v152", "v153", "v154", "v155", "v156", "v157", "v158", "v159", "v160", "v161", "v162", "v163", "v164", "v165", "v166", "v167", "v168", "v169", "v170", "v171", "v172", "v173", "v174", "v175", "v176", "v177", "v178", "v179", "v180", "v181", "v182", "v183", "v184", "v185", "v186", "v187", "v188", "v189", "v190", "v191", "v192", "v193", "v194", "v195", "v196", "v197", "v198", "v199", "v200", "v201", "v202", "v203", "v204", "v205", "v206", "v207", "v208", "v209", "v210", "v211", "v212", "v213", "v214", "v215", "v216", "v217", "v218", "v219", "v220", "v221", "v222", "v223", "v224", "v225", "v226", "v227", "v228", "v229", "v230", "v231", "v232", "v233", "v234", "v235", "v236", "v237", "v238", "v239", "v240", "v241", "v242", "v243", "v244", "v245", "v246", "v247", "v248", "v249", "v250", "v251", "v252", "v253", "v254", "v255", "a0", "a1", "a2", "a3", "a4", "a5", "a6", "a7", "a8", "a9", "a10", "a11", "a12", "a13", "a14", "a15", "a16", "a17", "a18", "a19", "a20", "a21", "a22", "a23", "a24", "a25", "a26", "a27", "a28", "a29", "a30", "a31", "a32", "a33", "a34", "a35", "a36", "a37", "a38", "a39", "a40", "a41", "a42", "a43", "a44", "a45", "a46", "a47", "a48", "a49", "a50", "a51", "a52", "a53", "a54", "a55", "a56", "a57", "a58", "a59", "a60", "a61", "a62", "a63", "a64", "a65", "a66", "a67", "a68", "a69", "a70", "a71", "a72", "a73", "a74", "a75", "a76", "a77", "a78", "a79", "a80", "a81", "a82", "a83", "a84", "a85", "a86", "a87", "a88", "a89", "a90", "a91", "a92", "a93", "a94", "a95", "a96", "a97", "a98", "a99", "a100", "a101", "a102", "a103", "a104", "a105", "a106", "a107", "a108", "a109", "a110", "a111", "a112", "a113", "a114", "a115", "a116", "a117", "a118", "a119", "a120", "a121", "a122", "a123", "a124", "a125", "a126", "a127", "a128", "a129", "a130", "a131", "a132", "a133", "a134", "a135", "a136", "a137", "a138", "a139", "a140", "a141", "a142", "a143", "a144", "a145", "a146", "a147", "a148", "a149", "a150", "a151", "a152", "a153", "a154", "a155", "a156", "a157", "a158", "a159", "a160", "a161", "a162", "a163", "a164", "a165", "a166", "a167", "a168", "a169", "a170", "a171", "a172", "a173", "a174", "a175", "a176", "a177", "a178", "a179", "a180", "a181", "a182", "a183", "a184", "a185", "a186", "a187", "a188", "a189", "a190", "a191", "a192", "a193", "a194", "a195", "a196", "a197", "a198", "a199", "a200", "a201", "a202", "a203", "a204", "a205", "a206", "a207", "a208", "a209", "a210", "a211", "a212", "a213", "a214", "a215", "a216", "a217", "a218", "a219", "a220", "a221", "a222", "a223", "a224", "a225", "a226", "a227", "a228", "a229", "a230", "a231", "a232", "a233", "a234", "a235", "a236", "a237", "a238", "a239", "a240", "a241", "a242", "a243", "a244", "a245", "a246", "a247", "a248", "a249", "a250", "a251", "a252", "a253", "a254", "a255", "s3", "s4", "s5", "s6", "s7", "s8", "s9", "s10", "s11", "s12", "s13", "s14", "s15", "s16", "s17", "s18", "s19", "s20", "s21", "s22", "s23", "s24", "s25", "s26", "s27", "s28", "s29", "s30", "s31", "s32", "s33", "s34", "s35", "s36", "s37", "s38", "s39", "s40", "s41", "s42", "s43", "s44", "s45", "s46", "s47", "s48", "s49", "s50", "s51", "s52", "s53", "s54", "s55", "s56", "s57", "s58", "s59", "s60", "s61", "s62", "s63", "s64", "s65", "s66", "s67", "s68", "s69", "s70", "s71", "s72", "s73", "s74", "s75", "s76", "s77", "s78", "s79", "s80", "s81", "s82", "s83", "s84", "s85", "s86", "s87", "s88", "s89", "s90", "s91", "s92", "s93", "s94", "s95", "s96", "s97", "s98", "s99", "vcc", "memory");
}

extern "C" void kernel_launch(void* const* d_in, const int* in_sizes, int n_in, void* d_out, int out_size, void* d_ws, size_t ws_size,
                              hipStream_t stream) {
    Args a;
    a.x = Buf{d_in[0], 2048u * 512u * 2u, 0x20000u};
    a.ws = Buf{d_ws, 14680064u, 0x20000u};
    a.w2 = Buf{d_in[11], 512u * 1792u * 2u, 0x20000u};
    a.w1 = Buf{d_in[10], 512u * 1792u * 2u, 0x20000u};
    a.out = Buf{d_out, 2048u * 512u * 2u, 0x20000u};
    for (int i = 0; i < 8; ++i) a.prm[i] = d_in[1 + i];
    (void)hipFuncSetAttribute((const void*)fused_kernel, hipFuncAttributeMaxDynamicSharedMemorySize, LDS_BYTES);
    fused_kernel<<<256, 256, LDS_BYTES, stream>>>(a);
}
```
